```python
import torch

def _softmax(x, axis=-1):
    return torch.softmax(x, dim=axis)
def _gelu(x):
    return torch.nn.functional.gelu(x, approximate="tanh")
def _silu(x):
    return torch.nn.functional.silu(x)
def _sigmoid(x):
    return torch.sigmoid(x)
def _layernorm(x, axis=-1, eps=1e-5):
    m = x.mean(dim=axis, keepdim=True)
    v = x.var(dim=axis, keepdim=True, unbiased=False)
    return (x - m) / torch.sqrt(v + eps)
def _scatter_add(tgt, idx, src, axis=0):
    return tgt.clone().index_add_(axis, idx, src)
def _segment_sum(src, seg, n):
    out = torch.zeros((n,) + src.shape[1:], dtype=src.dtype)
    return out.index_add_(0, seg, src)
def _pad(x, pw):
    flat = tuple(v for pair in reversed(pw) for v in pair)
    return torch.nn.functional.pad(x, flat)

kernel_name = 'interleave_layers__grouped_kv_mqa__glu_gated_geglu__prompt'

def setup_inputs():

    _w = (lambda a: (lambda t: t.to(torch.bfloat16) if t.is_floating_point() else t)(torch.as_tensor(a)))
    def _A_setup():
        B, S, D, H = 1, 2048, 512, 32
        Dh = D // H
        return {
            "x": _w((lambda *s: torch.randn(*s, dtype=torch.bfloat16) * 0.05)(B, S, D)),
            "wq": _w((lambda *s: torch.randn(*s, dtype=torch.bfloat16) * 0.05)(D, H * Dh)),
            "wk": _w((lambda *s: torch.randn(*s, dtype=torch.bfloat16) * 0.05)(D, Dh)),
            "wv": _w((lambda *s: torch.randn(*s, dtype=torch.bfloat16) * 0.05)(D, Dh)),
            "wo": _w((lambda *s: torch.randn(*s, dtype=torch.bfloat16) * 0.05)(H * Dh, D)),
        }
    _A_in = _A_setup()

    def _B_setup():
        B, S, Dm, Df = 1, 2048, 512, 1376
        return {
            "x": _w((lambda *s: torch.randn(*s, dtype=torch.bfloat16) * 0.05)(B, S, Dm)),
            "w_gate": _w((lambda *s: torch.randn(*s, dtype=torch.bfloat16) * 0.8)(Dm, Df) * (Dm ** -0.5)),
            "w_up": _w((lambda *s: torch.randn(*s, dtype=torch.bfloat16) * 0.8)(Dm, Df) * (Dm ** -0.5)),
            "w_down": _w((lambda *s: torch.randn(*s, dtype=torch.bfloat16) * 0.8)(Df, Dm) * (Df ** -0.5)),
        }
    _B_in = _B_setup()

    return {
        "A_x": _A_in["x"],
        "A_wq": _A_in["wq"],
        "A_wk": _A_in["wk"],
        "A_wv": _A_in["wv"],
        "A_wo": _A_in["wo"],
        "B_x": _B_in["x"],
        "B_w_gate": _B_in["w_gate"],
        "B_w_up": _B_in["w_up"],
        "B_w_down": _B_in["w_down"],
    }

def reference(A_x, A_wq, A_wk, A_wv, A_wo, B_x, B_w_gate, B_w_up, B_w_down):
    def _A_ref(x, wq, wk, wv, wo):
        B, S, D = x.shape
        H = 32
        Dh = D // H
        q = (lambda x, *d: x.permute(*d))((x @ wq).reshape(B, S, H, Dh), 0, 2, 1, 3)
        k = x @ wk
        v = x @ wv
        scores = torch.einsum("bhqd,bkd->bhqk", q, k) * (Dh ** -0.5)
        p = _softmax(scores, axis=-1)
        o = torch.einsum("bhqk,bkd->bhqd", p, v)
        return (lambda x, *d: x.permute(*d))(o, 0, 2, 1, 3).reshape(B, S, H * Dh) @ wo

    def _B_ref(x, w_gate, w_up, w_down):
        g = torch.einsum("bsd,df->bsf", x, w_gate)
        u = torch.einsum("bsd,df->bsf", x, w_up)
        h = _gelu(g) * u
        return torch.einsum("bsf,fd->bsd", h, w_down)

    x = A_x
    x = x + _A_ref(_layernorm(x, axis=-1), A_wq, A_wk, A_wv, A_wo)
    x = x + _B_ref(_layernorm(x, axis=-1), B_w_gate, B_w_up, B_w_down)
    x = x + _A_ref(_layernorm(x, axis=-1), A_wq, A_wk, A_wv, A_wo)
    x = x + _B_ref(_layernorm(x, axis=-1), B_w_gate, B_w_up, B_w_down)
    return x
```

```cpp
#include <hip/hip_runtime.h>
#include <hip/hip_bf16.h>
#include <stdint.h>

typedef float f32x2 __attribute__((ext_vector_type(2)));
typedef float f32x4 __attribute__((ext_vector_type(4)));
typedef float f32x16 __attribute__((ext_vector_type(16)));
typedef __bf16 bf16x2 __attribute__((ext_vector_type(2)));
typedef __bf16 bf16x8 __attribute__((ext_vector_type(8)));
typedef short s16x4 __attribute__((ext_vector_type(4)));
typedef unsigned u32x2 __attribute__((ext_vector_type(2)));
typedef unsigned u32x4 __attribute__((ext_vector_type(4)));
typedef unsigned long long u64;
typedef __amdgpu_buffer_rsrc_t rsrc_t;
#define DI __device__ __forceinline__
#define LAS __attribute__((address_space(3)))
#define GAS __attribute__((address_space(1)))
#define SB() __builtin_amdgcn_sched_barrier(0)
#define EXP2(x) __builtin_amdgcn_exp2f(x)

constexpr int SEQ = 2048, DM = 512, FF = 1376, NRB = 64, NFB = 86;
constexpr int VR = 576;
constexpr int TB = 1024 + 2 * VR;
constexpr int SLOTB = 16 * TB;
constexpr unsigned WQ_T = 0;
constexpr unsigned WKV_T = WQ_T + 16 * 32 * 1024;
constexpr unsigned WO_T = WKV_T + 32 * 1024;
constexpr unsigned WGU_T = WO_T + 16 * 32 * 1024;
constexpr unsigned WD_T = WGU_T + 86 * 32 * 1024;
constexpr unsigned CSGU = WD_T + 16 * 86 * 1024;
constexpr unsigned KV0 = (CSGU + 8 * 86 * 32 * 4 + 4095) / 4096 * 4096;
constexpr unsigned KVL = NRB * TB;
constexpr unsigned AOF = KV0 + 2 * KVL;
constexpr unsigned X1F = AOF + 2 * NRB * 32 * 1024;
constexpr unsigned HF = X1F + 2 * NRB * 32 * 1024;
constexpr unsigned X2F = HF + 2 * NRB * 86 * 1024;
constexpr unsigned FLG_PZ = X2F + NRB * 32 * 1024;
constexpr unsigned FLG_KV = FLG_PZ + NRB * 64;
constexpr unsigned FLG_T = FLG_KV + 2 * 8 * NRB * 16;
constexpr unsigned FLG_G = FLG_T + 512 * 8;
constexpr unsigned NONCE = FLG_G + 256 * 8;
constexpr unsigned DIAG = (NONCE + 128 + 4095) / 4096 * 4096;
constexpr unsigned WS_END = DIAG + 512 * 4 * 64 * 8;

constexpr unsigned L_A = 0, L_B = 32768;
constexpr unsigned L_TAB = 2 * SLOTB;
constexpr unsigned L_CSQ = L_TAB;
constexpr unsigned L_CSKV = L_CSQ + 256;
constexpr unsigned L_CSGU = L_CSKV + 128;
constexpr unsigned L_ST = L_CSGU + 11 * 128;
constexpr unsigned L_MISC = L_ST + 1024;
constexpr unsigned L_WKV = 65536;
constexpr unsigned L_DUMMY = 73728;
constexpr unsigned L_END = 81920;

DI f32x16 mfma32(u32x4 a, u32x4 b, f32x16 c) {
  return __builtin_amdgcn_mfma_f32_32x32x16_bf16(__builtin_bit_cast(bf16x8, a), __builtin_bit_cast(bf16x8, b), c, 0, 0, 0);
}
DI unsigned pk2(float a, float b) {
  f32x2 v = {a, b};
  return __builtin_bit_cast(unsigned, __builtin_convertvector(v, bf16x2));
}
DI float bflo(unsigned u) { return __uint_as_float(u << 16); }
DI float bfhi(unsigned u) { return __uint_as_float(u & 0xffff0000u); }
DI float sum32(float v) {
  auto r = __builtin_amdgcn_permlane32_swap(__float_as_uint(v), __float_as_uint(v), false, false);
  return __uint_as_float(r[0]) + __uint_as_float(r[1]);
}
DI float max32(float v) {
  auto r = __builtin_amdgcn_permlane32_swap(__float_as_uint(v), __float_as_uint(v), false, false);
  return fmaxf(__uint_as_float(r[0]), __uint_as_float(r[1]));
}
DI rsrc_t make_rsrc(const void* p, unsigned bytes) { return __builtin_amdgcn_make_buffer_rsrc((void*)p, 0, bytes, 0x00020000); }
constexpr int SC1 = 16;
template <int AUX> DI u32x4 ld16(rsrc_t r, unsigned voff, unsigned soff) { return __builtin_amdgcn_raw_buffer_load_b128(r, voff, soff, AUX); }
template <int AUX> DI void st16(rsrc_t r, u32x4 v, unsigned voff, unsigned soff) {
  __builtin_amdgcn_raw_buffer_store_b128(v, r, voff, soff, AUX);
  asm volatile("s_nop 1" :: "v"(v));
}
DI u32x2 ld8(rsrc_t r, unsigned voff, unsigned soff) { return __builtin_amdgcn_raw_buffer_load_b64(r, voff, soff, 0); }
template <int AUX> DI void glds16(rsrc_t r, unsigned voff, unsigned soff, unsigned ldsaddr) {
  __builtin_amdgcn_raw_ptr_buffer_load_lds(r, (LAS void*)(size_t)ldsaddr, 16, voff, soff, 0, AUX);
}
DI float dot2(unsigned a, unsigned b, float c) {
  return __builtin_amdgcn_fdot2_f32_bf16(__builtin_bit_cast(bf16x2, a), __builtin_bit_cast(bf16x2, b), c, false);
}
constexpr unsigned ONES2 = 0x3f803f80u;
DI unsigned opq(unsigned x) { x = __builtin_amdgcn_readfirstlane(x); asm volatile("" : "+s"(x)); return x; }
DI u32x2 tr8(unsigned ldsaddr) {
  return __builtin_bit_cast(u32x2, __builtin_amdgcn_ds_read_tr16_b64_v4i16((LAS s16x4*)(size_t)ldsaddr));
}
DI u32x4 tr_frag(unsigned a, unsigned row_stride) {
  const u32x2 lo = tr8(a), hi = tr8(a + 8 * row_stride);
  return (u32x4){lo[0], lo[1], hi[0], hi[1]};
}
DI u32x4 tr_frag_nat(unsigned a, unsigned row_stride) {
  const u32x2 lo = tr8(a), hi = tr8(a + 4 * row_stride);
  return (u32x4){lo[0], lo[1], hi[0], hi[1]};
}
DI float frag_sum(u32x4 f, float s) {
#pragma unroll
  for (int e = 0; e < 4; ++e) s = dot2(f[e], ONES2, s);
  return s;
}
#define WAITV0() asm volatile("s_waitcnt vmcnt(0)" ::: "memory")
#define BARRIER() asm volatile("s_waitcnt lgkmcnt(0)\n\ts_barrier" ::: "memory")

DI void decode_block(int b, int& R, int& j) {
  const int x = b & 7, r = b >> 3;
  j = r & 7;
  R = x * 8 + (r >> 3);
}
DI int kvpos(int R) { return ((R & 4) << 3) + ((R >> 3) << 2) + (R & 3); }

template <int NT> DI void red_write(char* lds, int w, int lane, const f32x16* acc) {
#pragma unroll
  for (int t = 0; t < NT; ++t)
#pragma unroll
    for (int q = 0; q < 4; ++q)
      *(f32x4*)(lds + (((w * NT + t) * 4 + q) * 64 + lane) * 16) = (f32x4){acc[t][4 * q], acc[t][4 * q + 1], acc[t][4 * q + 2], acc[t][4 * q + 3]};
}
template <int NT> DI void red_read8(const char* lds, int t, int s, int lane, float* v) {
#pragma unroll
  for (int qq = 0; qq < 2; ++qq) {
    f32x4 a = *(const f32x4*)(lds + (((0 * NT + t) * 4 + 2 * s + qq) * 64 + lane) * 16);
#pragma unroll
    for (int w = 1; w < 4; ++w) a += *(const f32x4*)(lds + (((w * NT + t) * 4 + 2 * s + qq) * 64 + lane) * 16);
    v[4 * qq] = a[0]; v[4 * qq + 1] = a[1]; v[4 * qq + 2] = a[2]; v[4 * qq + 3] = a[3];
  }
}
template <int NT, int WS> DI void red_write_s(char* lds, int w, int lane, const f32x16* acc) {
#pragma unroll
  for (int t = 0; t < NT; ++t)
#pragma unroll
    for (int q = 0; q < 4; ++q)
      *(f32x4*)(lds + w * WS + ((t * 4 + q) * 64 + lane) * 16) = (f32x4){acc[t][4 * q], acc[t][4 * q + 1], acc[t][4 * q + 2], acc[t][4 * q + 3]};
}
template <int WS> DI void red_read8_s(const char* lds, int t, int s, int lane, float* v) {
#pragma unroll
  for (int qq = 0; qq < 2; ++qq) {
    f32x4 a = *(const f32x4*)(lds + ((t * 4 + 2 * s + qq) * 64 + lane) * 16);
#pragma unroll
    for (int w = 1; w < 4; ++w) a += *(const f32x4*)(lds + w * WS + ((t * 4 + 2 * s + qq) * 64 + lane) * 16);
    v[4 * qq] = a[0]; v[4 * qq + 1] = a[1]; v[4 * qq + 2] = a[2]; v[4 * qq + 3] = a[3];
  }
}
constexpr unsigned POISON = 0xffffffffu;
DI u32x4 poison4() { return (u32x4){POISON, POISON, POISON, POISON}; }
DI u32x4 unpoison(u32x4 v) { return (u32x4){min(v[0], POISON - 1), min(v[1], POISON - 1), min(v[2], POISON - 1), min(v[3], POISON - 1)}; }
template <int N> DI bool frags_ok(const u32x4* b) {
  unsigned m = 0;
#pragma unroll
  for (int i = 0; i < N; ++i) m = max(max(max(m, b[i][0]), max(b[i][1], b[i][2])), b[i][3]);
  return __all(m != POISON);
}
template <int SS> DI void ln_stats_s(const f32x2* st, int tok, float& mean, float& rstd) {
  const f32x2 a = st[tok] + st[SS + tok] + st[2 * SS + tok] + st[3 * SS + tok];
  mean = a[0] * (1.f / 512.f);
  rstd = __builtin_amdgcn_rsqf(fmaxf(a[1] * (1.f / 512.f) - mean * mean, 0.f) + 1e-5f);
}
DI void ln_stats(const f32x2* st, int tok, float& mean, float& rstd) {
  const f32x2 a = st[tok] + st[32 + tok] + st[64 + tok] + st[96 + tok];
  mean = a[0] * (1.f / 512.f);
  rstd = __builtin_amdgcn_rsqf(fmaxf(a[1] * (1.f / 512.f) - mean * mean, 0.f) + 1e-5f);
}
DI u32x4 ln_pack(const float* v, f32x4 c0, f32x4 c1, float mean, float sc) {
  return (u32x4){pk2((v[0] - mean * c0[0]) * sc, (v[1] - mean * c0[1]) * sc), pk2((v[2] - mean * c0[2]) * sc, (v[3] - mean * c0[3]) * sc),
                 pk2((v[4] - mean * c1[0]) * sc, (v[5] - mean * c1[1]) * sc), pk2((v[6] - mean * c1[2]) * sc, (v[7] - mean * c1[3]) * sc)};
}
constexpr float QSCALE = 0.25f * 1.4426950408889634f;

template <int N> DI void set_tag(char* ws, unsigned off, u64 tag, int lane) {
  asm volatile("s_waitcnt vmcnt(%0)" :: "n"(N) : "memory");
  if (lane == 0) __hip_atomic_store((GAS u64*)(ws + off), tag, __ATOMIC_RELAXED, __HIP_MEMORY_SCOPE_AGENT);
}
constexpr unsigned SPIN_CAP = 1u << 17;
DI void wait_tags(const char* ws, unsigned off, bool active, u64 tag) {
  const GAS u64* p = (const GAS u64*)(ws + off);
  for (unsigned spins = 0; spins < SPIN_CAP; ++spins) {
    const u64 v = active ? __hip_atomic_load(p, __ATOMIC_RELAXED, __HIP_MEMORY_SCOPE_AGENT) : tag;
    if (__all(v == tag)) return;
    __builtin_amdgcn_s_sleep(8);
  }
}
DI bool pair_ok(u32x4 v, u64 tag) { return v[0] == (unsigned)tag && v[1] == (unsigned)(tag >> 32) && v[2] == (unsigned)tag && v[3] == (unsigned)(tag >> 32); }
DI void wait_pairs(rsrc_t rs, unsigned voff, unsigned soff, bool active, u64 tag) {
  for (unsigned spins = 0; spins < SPIN_CAP; ++spins) {
    asm volatile("" ::: "memory");
    const u32x4 v = ld16<SC1>(rs, voff, soff);
    if (__all(!active || pair_ok(v, tag))) return;
    __builtin_amdgcn_s_sleep(4);
  }
}
DI void kv_publish(char* ws, int layer, int pos, int role, u64 tag, int lane) {
  if (lane < 8) __hip_atomic_store((GAS u64*)(ws + FLG_KV) + ((layer * 8 + lane) * 64 + pos) * 2 + (role - 1), tag, __ATOMIC_RELAXED, __HIP_MEMORY_SCOPE_AGENT);
}
DI float geglu(float g, float u) {
  const float z = g * __builtin_fmaf(g * g, -0.044715f * 2.f * 0.7978845608028654f * 1.4426950408889634f, -2.f * 0.7978845608028654f * 1.4426950408889634f);
  return g * u * __builtin_amdgcn_rcpf(1.f + EXP2(z));
}

DI void prepoll(rsrc_t rs, unsigned soff, int n, int lane) {
  const unsigned po = min(lane, n - 1) * 1024 + 1020;
  for (unsigned spins = 0; spins < SPIN_CAP; ++spins) {
    asm volatile("" ::: "memory");
    const unsigned v = __builtin_amdgcn_raw_buffer_load_b32(rs, po, soff, SC1);
    if (__all(v != POISON)) return;
    __builtin_amdgcn_s_sleep(1);
  }
}
template <int N> DI void poll_frags(rsrc_t rs, unsigned voff, unsigned soff, u32x4* b) {
  prepoll(rs, soff, N, voff >> 4);
  for (unsigned spins = 0; spins < SPIN_CAP; ++spins) {
    asm volatile("" ::: "memory");
#pragma unroll
    for (int i = 0; i < N; ++i) b[i] = ld16<SC1>(rs, voff, soff + i * 1024);
    if (frags_ok<N>(b)) return;
    __builtin_amdgcn_s_sleep(2);
  }
}
template <int N> DI void load_frags(rsrc_t rs, unsigned voff, unsigned soff, u32x4* b) {
  for (unsigned spins = 0; spins < SPIN_CAP; ++spins) {
    asm volatile("" ::: "memory");
#pragma unroll
    for (int i = 0; i < N; ++i) b[i] = ld16<SC1>(rs, voff, soff + i * 1024);
    if (frags_ok<N>(b)) return;
    __builtin_amdgcn_s_sleep(2);
  }
}

template <class RD, class CS>
DI void kv_finish(rsrc_t rs, int role, RD rd, float mean, float rstd, CS cs, unsigned kvbase, int lane, char* scr) {
  const int hi = lane >> 5, t = lane & 31;
  if (role == 1) {
    float v[8]; rd(0, v);
    const f32x4 c0 = {cs(4 * hi), cs(4 * hi + 1), cs(4 * hi + 2), cs(4 * hi + 3)}, c1 = {cs(8 + 4 * hi), cs(9 + 4 * hi), cs(10 + 4 * hi), cs(11 + 4 * hi)};
    st16<SC1>(rs, ln_pack(v, c0, c1, mean, rstd), lane * 16, kvbase);
  } else {
    float* sm = (float*)scr;
    if (hi == 0) { sm[t] = rstd; sm[32 + t] = mean * rstd; }
    asm volatile("s_waitcnt lgkmcnt(0)" ::: "memory");
    const float c = cs(t & 15);
#pragma unroll
    for (int s = 0; s < 2; ++s) {
      float v[8]; rd(s, v);
      const f32x4 r0 = *(const f32x4*)(sm + 16 * s + 4 * hi), r1 = *(const f32x4*)(sm + 16 * s + 8 + 4 * hi);
      const f32x4 m0 = *(const f32x4*)(sm + 32 + 16 * s + 4 * hi), m1 = *(const f32x4*)(sm + 32 + 16 * s + 8 + 4 * hi);
      u32x4 o = {pk2(v[0] * r0[0] - m0[0] * c, v[1] * r0[1] - m0[1] * c), pk2(v[2] * r0[2] - m0[2] * c, v[3] * r0[3] - m0[3] * c),
                 pk2(v[4] * r1[0] - m1[0] * c, v[5] * r1[1] - m1[1] * c), pk2(v[6] * r1[2] - m1[2] * c, v[7] * r1[3] - m1[3] * c)};
      if (t >= 16) { const unsigned k = t == 16 ? ONES2 : 0u; o = (u32x4){k, k, k, k}; }
      if (t < 18) st16<SC1>(rs, o, hi * (VR / 2) + t * 16, kvbase + 1024 + s * VR);
    }
  }
}
DI void stats_of(f32x2 a, float& mean, float& rstd) {
  mean = a[0] * (1.f / 512.f);
  rstd = __builtin_amdgcn_rsqf(fmaxf(a[1] * (1.f / 512.f) - mean * mean, 0.f) + 1e-5f);
}

#define STAMP(i) do { } while (0)
#define CSTAMP(i) do { } while (0)
#define STAMP0N(i) do { } while (0)
#define STAMPV(i, v) do { } while (0)
#define STAMPG(i) do { if ((threadIdx.x & 63) == 0) ((unsigned long long*)(ws + DIAG))[blockIdx.x * 256 + (threadIdx.x >> 6) * 64 + (i)] = __builtin_amdgcn_s_memrealtime(); } while (0)
#define CSTAMPG(i) do { if ((threadIdx.x & 63) == 0) ((unsigned long long*)(ws + DIAG))[blockIdx.x * 256 + (threadIdx.x >> 6) * 64 + (i)] = __builtin_amdgcn_s_memtime(); } while (0)
#define STAMPINIT() do { } while (0)
#define STAMPG(i) do { } while (0)
#define CSTAMPG(i) do { } while (0)
#define STAMPDUMP() do { } while (0)

#define STAGE_VARS()                                                                                              \
  int tid = threadIdx.x; asm volatile("" : "+v"(tid));                                                            \
  int R = R_, j = j_, w = w_; asm volatile("" : "+s"(R), "+s"(j), "+s"(w));                                       \
  const int lane = tid & 63, hi = lane >> 5, t = lane & 31, hp = w & 1, kh = w >> 1;                              \
  const unsigned l16 = lane * 16;                                                                                 \
  const int nfb = j < 6 ? 11 : 10, fb0 = j < 6 ? 11 * j : 66 + 10 * (j - 6);                                      \
  const int role = j == (R & 7) ? 1 : (j == ((R + 1) & 7) ? 2 : 0);      \
  (void)hi; (void)t; (void)hp; (void)kh; (void)l16; (void)nfb; (void)fb0; (void)R; (void)role;

__global__ __launch_bounds__(256, 2) void k_main(const uint16_t* __restrict__ x, const uint16_t* __restrict__ wq,
                                                 const uint16_t* __restrict__ wk, const uint16_t* __restrict__ wv,
                                                 const uint16_t* __restrict__ wo, const uint16_t* __restrict__ wg,
                                                 const uint16_t* __restrict__ wu, const uint16_t* __restrict__ wd,
                                                 char* __restrict__ ws, uint16_t* __restrict__ out) {
  extern __shared__ __attribute__((aligned(16))) char lds[];
  int R_, j_; decode_block(blockIdx.x, R_, j_);
  const int w_ = __builtin_amdgcn_readfirstlane(threadIdx.x >> 6);
  const rsrc_t rs = make_rsrc(ws, WS_END);
  const unsigned ldsb = (unsigned)(size_t)(LAS char*)lds;
  const bool strm_b = (R_ & 4) != 0;
  const f32x2* st = (const f32x2*)(lds + L_ST);
  u32x4 q0, q1;
  u32x4 xr;
  u64 tag;
#define DMA(P, GOFF, SLOT) do { if ((P) < 8 || w < 2) glds16<SC1>(rs, l16, (GOFF) + w * 1024 + (P) * 4096, ldsb + (SLOT) * SLOTB + w * 1024 + (P) * 4096); } while (0)
  {
    STAGE_VARS();
    STAMPG(0);
    CSTAMPG(30);
    if (role) __builtin_amdgcn_s_setprio(3);
    const u32x2 nv = *(const GAS u32x2*)(ws + NONCE);
    if (role) {
      const rsrc_t rk = make_rsrc(role == 1 ? wk : wv, DM * 16 * 2);
#pragma unroll
      for (int n = 0; n < 4; ++n) glds16<0>(rk, l16, (128 * w + 32 * n) * 32, ldsb + L_WKV + (128 * w + 32 * n) * 32);
    }
    u32x4 xb[8];
    {
      const rsrc_t rx = make_rsrc(x, SEQ * DM * 2);
#pragma unroll
      for (int i = 0; i < 8; ++i) xb[i] = ld16<0>(rx, t * 1024 + hi * 16, R * 32768 + (8 * w + i) * 32);
    }
    WAITV0();
    {
      const rsrc_t rq = make_rsrc(wq, DM * DM * 2);
#pragma unroll
      for (int n = 0; n < 16; ++n)
        glds16<0>(rq, (lane >> 3) * 1024 + (lane & 7) * 16, (128 * w + 8 * n) * 1024 + j * 128, ldsb + (128 * w + 8 * n) * 128);
    }
    SB();
    tag = (((u64)__builtin_amdgcn_readfirstlane(nv[1]) << 32) | (u64)__builtin_amdgcn_readfirstlane(nv[0])) + 1;
    STAMPG(25);
    const unsigned trl = (8 * hi + ((lane & 15) >> 2)) * 128 + (((lane >> 4) & 1) * 16 + (lane & 3) * 4) * 2;
    const unsigned trk = (8 * hi + ((lane & 15) >> 2)) * 32 + (lane & 3) * 8;
    const unsigned trlp = trl - 4 * hi * 128, trkp = trk - 4 * hi * 32;
    const int bq = strm_b ? kvpos(R) - 32 : -1;
    char* const qw = lds + L_WKV + w * 4096;
    float s1 = 0.f, s2 = 0.f;
#pragma unroll
    for (int i = 0; i < 8; ++i)
#pragma unroll
      for (int e = 0; e < 4; ++e) { s1 = dot2(xb[i][e], ONES2, s1); s2 = dot2(xb[i][e], xb[i][e], s2); }
    s1 = sum32(s1); s2 = sum32(s2);
    if (role) {
      f32x16 akv = {};
      float cskv = 0.f;
      u32x4 kf[8];
#pragma unroll
      for (int i = 0; i < 8; ++i) { kf[i] = tr_frag_nat(ldsb + L_WKV + (8 * w + i) * 16 * 32 + trk, 32); cskv = frag_sum(kf[i], cskv); }
      if (role == 1) {
#pragma unroll
        for (int i = 0; i < 8; ++i) akv = mfma32(kf[i], xb[i], akv);
      } else {
#pragma unroll
        for (int i = 0; i < 8; ++i) akv = mfma32(xb[i], kf[i], akv);
      }
      if (R == 4) {
#pragma unroll
        for (int i = 0; i < 8; ++i) {
          const u32x4 ap = tr_frag(ldsb + L_WKV + (8 * w + i) * 16 * 32 + trkp, 32);
          if (((lane >> 4) & 1) == role - 1) st16<SC1>(rs, ap, l16, WKV_T + (8 * w + i) * 1024);
        }
      }
      cskv = sum32(cskv);
      asm volatile("s_waitcnt lgkmcnt(0)" ::: "memory");
      if (role == 1) {
        *(f32x4*)(qw + l16) = (f32x4){akv[0], akv[1], akv[2], akv[3]};
        *(f32x4*)(qw + 1024 + l16) = (f32x4){akv[4], akv[5], akv[6], akv[7]};
      } else if (t < 16) {
#pragma unroll
        for (int q = 0; q < 4; ++q) *(f32x4*)(qw + ((q * 2 + hi) * 16 + t) * 16) = (f32x4){akv[4 * q], akv[4 * q + 1], akv[4 * q + 2], akv[4 * q + 3]};
      }
      if (hi == 0) ((float*)(qw + 2304))[t] = cskv;
    }
    if (hi == 0) ((f32x2*)(qw + 2048))[t] = (f32x2){s1, s2};
    BARRIER();
    auto stat = [&](int tok, float& mean, float& rstd) {
      const char* p = lds + L_WKV + 2048 + tok * 8;
      stats_of(*(const f32x2*)p + *(const f32x2*)(p + 4096) + *(const f32x2*)(p + 8192) + *(const f32x2*)(p + 12288), mean, rstd);
    };
    auto cskv4 = [&](int i) { const float* p = (const float*)(lds + L_WKV + 2304) + i; return p[0] + p[1024] + p[2048] + p[3072]; };
    if (role && w == role - 1) {
      auto rd = [&](int s, float* v) {
#pragma unroll
        for (int qq = 0; qq < 2; ++qq) {
          const char* p = lds + L_WKV + (role == 1 ? qq * 1024 + l16 : (((2 * s + qq) * 2 + hi) * 16 + (t & 15)) * 16);
          const f32x4 a = *(const f32x4*)p + *(const f32x4*)(p + 4096) + *(const f32x4*)(p + 8192) + *(const f32x4*)(p + 12288);
          v[4 * qq] = a[0]; v[4 * qq + 1] = a[1]; v[4 * qq + 2] = a[2]; v[4 * qq + 3] = a[3];
        }
      };
      float mean, rstd; stat(t, mean, rstd);
      kv_finish(rs, role, rd, mean, rstd, cskv4, KV0 + kvpos(R) * TB, lane, qw + 3072);
      WAITV0();
      kv_publish(ws, 0, kvpos(R), role, tag, lane);
    }
    STAMPG(26);
    WAITV0();
    {
      const rsrc_t rx = make_rsrc(x, SEQ * DM * 2);
      const u32x2 a = ld8(rx, t * 1024 + hi * 8, R * 32768 + (4 * j + w) * 32), b = ld8(rx, t * 1024 + hi * 8 + 16, R * 32768 + (4 * j + w) * 32);
      xr = (u32x4){a[0], a[1], b[0], b[1]};
    }
    f32x16 acc[2] = {};
    float csq0 = 0.f, csq1 = 0.f;
#pragma unroll
    for (int i = 0; i < 8; ++i) {
      const int ks = 8 * w + i;
      const u32x4 a0 = tr_frag_nat(ldsb + ks * 16 * 128 + trl, 128), a1 = tr_frag_nat(ldsb + ks * 16 * 128 + 64 + trl, 128);
      acc[0] = mfma32(a0, xb[i], acc[0]);
      acc[1] = mfma32(a1, xb[i], acc[1]);
      csq0 = frag_sum(a0, csq0); csq1 = frag_sum(a1, csq1);
      if (bq == ks) {
        st16<SC1>(rs, tr_frag(ldsb + ks * 16 * 128 + trlp, 128), l16, WQ_T + ((2 * j) * 32 + ks) * 1024);
        st16<SC1>(rs, tr_frag(ldsb + ks * 16 * 128 + 64 + trlp, 128), l16, WQ_T + ((2 * j + 1) * 32 + ks) * 1024);
      }
    }
    csq0 = sum32(csq0); csq1 = sum32(csq1);
    asm volatile("s_waitcnt lgkmcnt(0)" ::: "memory");
    red_write_s<2, 16384>(lds, w, lane, acc);
    if (hi == 0) { ((float*)(lds + w * 16384 + 8192))[t] = csq0; ((float*)(lds + w * 16384 + 8192))[32 + t] = csq1; }
    BARRIER();
    {
      float mean, rstd; stat(t, mean, rstd);
      f32x4 c0 = {}, c1 = {}, c2 = {}, c3 = {};
#pragma unroll
      for (int p = 0; p < 4; ++p) {
        const float* c = (const float*)(lds + p * 16384 + 8192) + 32 * hp + 4 * hi;
        c0 += *(const f32x4*)c; c1 += *(const f32x4*)(c + 8); c2 += *(const f32x4*)(c + 16); c3 += *(const f32x4*)(c + 24);
      }
      float v[8];
      red_read8_s<16384>(lds, hp, 0, lane, v); q0 = ln_pack(v, c0, c1, mean, rstd * QSCALE);
      red_read8_s<16384>(lds, hp, 1, lane, v); q1 = ln_pack(v, c2, c3, mean, rstd * QSCALE);
    }
    float tabv = 0.f;
    if (tid < 64) { const float* c = (const float*)(lds + 8192) + tid; tabv = c[0] + c[4096] + c[8192] + c[12288]; }
    else if (tid < 96 && role) tabv = cskv4(tid - 64);
    BARRIER();
    if (tid < 96) ((float*)(lds + L_TAB))[tid] = tabv;
    STAMPG(27);
    __builtin_amdgcn_s_setprio(0);
    if (strm_b) {
      const u32x4 pz = poison4();
#pragma unroll
      for (int l = 0; l < 2; ++l) {
        st16<SC1>(rs, pz, l16, AOF + ((l * 64 + R) * 32 + 4 * j + 2 * hp + kh) * 1024);
        st16<SC1>(rs, pz, l16, X1F + ((l * 64 + R) * 32 + 4 * j + w) * 1024);
#pragma unroll
        for (int i = 0; i < 3; ++i) st16<SC1>(rs, pz, l16, (w + 4 * i < nfb ? 0u : 0x80000000u) | (HF + ((l * 64 + R) * 86 + fb0 + w + 4 * i) * 1024));
      }
      st16<SC1>(rs, pz, l16, X2F + (R * 32 + 4 * j + w) * 1024);
#pragma unroll 1
      for (int job = blockIdx.x - 256; job < 296; job += 256) {
        int jn = 0;
        unsigned jdstv = l16, jdst = 0, jcs = 0, jld = DM * 2, jvo = 0;
        const uint16_t* jbase = wo; unsigned jbytes = DM * DM * 2;
        if (job < 176) {
          const int kb = job & 7, up = w & 1, fb = 4 * (job >> 3) + 2 * (w >> 1);
          if (fb < NFB) {
            jn = 4; jld = FF * 2; jbase = up ? wu : wg; jbytes = DM * FF * 2;
            jvo = (64 * kb + 4 * hi) * (FF * 2) + fb * 32 + t * 2;
            jdst = WGU_T + (fb * 32 + 4 * kb) * 1024; jdstv = (t >> 4) * 32768 + (32 * hi + 16 * up + (t & 15)) * 16;
            jcs = CSGU + ((kb * 86 + fb + (t >> 4)) * 32 + 16 * up + (t & 15)) * 4;
          }
        } else {
          const bool o = job < 208;
          const int q = o ? job - 176 : job - 208, ct = 2 * (q & 7) + (w & 1), ks0 = 8 * (q >> 3) + 4 * (w >> 1), nks = o ? 32 : 86;
          jn = max(0, min(4, nks - ks0)); jbase = o ? wo : wd; jbytes = o ? DM * DM * 2 : FF * DM * 2;
          jvo = (16 * ks0 + 4 * hi) * (DM * 2) + ct * 64 + t * 2;
          jdst = (o ? WO_T : WD_T) + (ct * nks + ks0) * 1024;
        }
        const rsrc_t jrs = make_rsrc(jbase, jbytes);
        unsigned short jr[32];
#pragma unroll
        for (int i = 0; i < 32; ++i)
          jr[i] = __builtin_amdgcn_raw_buffer_load_b16(jrs, jvo + (16 * (i >> 3) + 8 * ((i & 7) >> 2) + (i & 3)) * jld, 0, 0);
        float cs = 0.f;
#pragma unroll
        for (int f = 0; f < 4; ++f) {
          if (f < jn) {
            const u32x4 a = {jr[8 * f] | ((unsigned)jr[8 * f + 1] << 16), jr[8 * f + 2] | ((unsigned)jr[8 * f + 3] << 16),
                             jr[8 * f + 4] | ((unsigned)jr[8 * f + 5] << 16), jr[8 * f + 6] | ((unsigned)jr[8 * f + 7] << 16)};
            st16<SC1>(rs, a, jdstv, jdst + f * 1024);
            cs = frag_sum(a, cs);
          }
        }
        if (jn && jld == FF * 2) {
          cs = sum32(cs);
          if (hi == 0) __hip_atomic_store((GAS float*)(ws + jcs), cs, __ATOMIC_RELAXED, __HIP_MEMORY_SCOPE_AGENT);
        }
      }
      WAITV0();
      BARRIER();
      if (tid == 0) __hip_atomic_store((GAS u64*)(ws + FLG_T) + blockIdx.x, tag, __ATOMIC_RELAXED, __HIP_MEMORY_SCOPE_AGENT);
    }
    STAMPG(28);
    if (w == 0) wait_pairs(rs, l16, FLG_KV + (blockIdx.x & 7) * 1024, true, tag);
    BARRIER();
    STAMPG(29);
#pragma unroll
    for (int p = 0; p < 9; ++p) DMA(p, KV0, 0);
    if (strm_b) {
      if (w == 0) wait_tags(ws, FLG_G + (blockIdx.x - 256) * 8, lane == 0, tag);
    }
  }

  STAMPINIT();
#pragma unroll 1
  for (int layer = 0; layer < 2; ++layer) {
    const unsigned kvb = KV0 + layer * KVL;
    u32x4 wof_[16];
    f32x16 o0 = {}, o1 = {};
    {
      STAGE_VARS();
      if (strm_b) __builtin_amdgcn_s_setprio(0); else __builtin_amdgcn_s_setprio(1);
      asm volatile("s_waitcnt vmcnt(0) lgkmcnt(0)\n\ts_barrier" ::: "memory");
      STAMP(1 + 12 * layer);
#pragma unroll
      for (int p = 0; p < 9; ++p) DMA(p, kvb + SLOTB, 1);
      if (layer == 0 && !strm_b) {
        const u32x4 pz = poison4();
#pragma unroll
        for (int l = 0; l < 2; ++l) {
          st16<SC1>(rs, pz, l16, AOF + ((l * 64 + R) * 32 + 4 * j + 2 * hp + kh) * 1024);
          st16<SC1>(rs, pz, l16, X1F + ((l * 64 + R) * 32 + 4 * j + w) * 1024);
#pragma unroll
          for (int i = 0; i < 3; ++i) st16<SC1>(rs, pz, l16, (w + 4 * i < nfb ? 0u : 0x80000000u) | (HF + ((l * 64 + R) * 86 + fb0 + w + 4 * i) * 1024));
        }
        st16<SC1>(rs, pz, l16, X2F + (R * 32 + 4 * j + w) * 1024);
      }
      SB();
      const int tc = t < 16 ? t : (t == 16 ? 16 : 17);
      const LAS u32x4* kp = (const LAS u32x4*)(size_t)(ldsb + kh * 8 * TB + l16);
      const LAS u32x4* vp = (const LAS u32x4*)(size_t)(ldsb + kh * 8 * TB + 1024 + hi * (VR / 2) + tc * 16);
      const f32x16 z = {};
      f32x16 sA, sB;
      u32x4 kX, vX0, vX1, kY, vY0, vY1, p0, p1;
      kX = kp[0]; vX0 = vp[0]; vX1 = vp[VR / 16];
      sA = mfma32(kX, q0, z);
      vY1 = vX1;
      p1 = (u32x4){0, 0, 0, 0};
      float e14 = 0.f, e15 = 0.f;
#define UNIT(SC, SN, KN, QN, OP, VP1, OC, VC0, A1, A2, A3)                                                               \
  do {                                                                                                                   \
    SN = mfma32(KN, QN, z); SB();                                                                                        \
    A1;                                                                                                                  \
    float e0 = EXP2(SC[0]), e1 = EXP2(SC[1]); SB();                                                                      \
    p1[3] = pk2(e14, e15); SB();                                                                                         \
    float e2 = EXP2(SC[2]), e3 = EXP2(SC[3]); SB();                                                                      \
    OP = mfma32(VP1, p1, OP); SB();                                                                                      \
    A2;                                                                                                                  \
    p0[0] = pk2(e0, e1); SB();                                                                                           \
    float e4 = EXP2(SC[4]), e5 = EXP2(SC[5]); SB();                                                                      \
    p0[1] = pk2(e2, e3); SB();                                                                                           \
    float e6 = EXP2(SC[6]), e7 = EXP2(SC[7]); SB();                                                                      \
    p0[2] = pk2(e4, e5); SB();                                                                                           \
    float e8 = EXP2(SC[8]), e9 = EXP2(SC[9]); SB();                                                                      \
    p0[3] = pk2(e6, e7); SB();                                                                                           \
    float e10 = EXP2(SC[10]), e11 = EXP2(SC[11]); SB();                                                                  \
    OC = mfma32(VC0, p0, OC); SB();                                                                                      \
    A3;                                                                                                                  \
    p1[0] = pk2(e8, e9); SB();                                                                                           \
    float e12 = EXP2(SC[12]), e13 = EXP2(SC[13]); SB();                                                                  \
    p1[1] = pk2(e10, e11); SB();                                                                                         \
    e14 = EXP2(SC[14]); e15 = EXP2(SC[15]); SB();                                                                        \
    p1[2] = pk2(e12, e13); SB();                                                                                         \
  } while (0)
#define LDN(KN, VN0, VN1, NOFF) do { KN = kp[(NOFF) / 16]; VN0 = vp[(NOFF) / 16]; VN1 = vp[((NOFF) + VR) / 16]; SB(); } while (0)
#define TILE(KC, VC0, VC1, KN, VN0, VN1, NOFF, X)                               \
    UNIT(sA, sB, KC, q1, o1, VN1, o0, VC0, , LDN(KN, VN0, VN1, NOFF), );        \
    UNIT(sB, sA, KN, q0, o0, VC1, o1, VC0, X, , );
#define PINP() asm volatile("" : "+v"(p1), "+v"(e14), "+v"(e15), "+v"(sA), "+v"(o0), "+v"(o1))
#define DM(P) do { glds16<SC1>(rs, l16, goff + (P) * 4096, (P) < 8 ? dsl + (P) * 4096 : dsl8); SB(); } while (0)
      int sl = 0;
      u32x4 fl; asm volatile("" : "=v"(fl));
      bool late = false;
#pragma unroll 1
      for (int c = 0; c < 4; ++c) {
        const bool chk = layer == 0 ? (c == 3) : (c == 0 && w == 0);
        const unsigned chks = chk ? 0u : 0x80000000u;
        const unsigned chko = layer == 0 ? FLG_T + w * 1024 + l16 : FLG_KV + (8 + (blockIdx.x & 7)) * 1024 + 512 + (l16 & 0x1f0);
#define CHKLD() do { fl = ld16<SC1>(rs, chko, chks); SB(); } while (0)
        TILE(kX, vX0, vX1, kY, vY0, vY1, 1 * TB, );
        TILE(kY, vY0, vY1, kX, vX0, vX1, 2 * TB, );
        TILE(kX, vX0, vX1, kY, vY0, vY1, 3 * TB, );
        TILE(kY, vY0, vY1, kX, vX0, vX1, 4 * TB, CHKLD());
        TILE(kX, vX0, vX1, kY, vY0, vY1, 5 * TB, );
        TILE(kY, vY0, vY1, kX, vX0, vX1, 6 * TB, );
        TILE(kX, vX0, vX1, kY, vY0, vY1, 7 * TB, );
        PINP();
        if (chk) {
          const bool ok = __all(pair_ok(fl, tag));
          if (layer == 0) { if (!ok) wait_pairs(rs, chko, 0, true, tag); }
          else if (tid == 0) *(volatile LAS unsigned*)(size_t)(ldsb + L_MISC) = ok ? 1u : 0u;
        }
        asm volatile("s_waitcnt vmcnt(0) lgkmcnt(0)\n\ts_barrier" ::: "memory");
        if (layer == 0 && c == 1 && tid == 0 && !strm_b) {
          __hip_atomic_store((GAS u64*)(ws + FLG_T) + blockIdx.x, tag, __ATOMIC_RELAXED, __HIP_MEMORY_SCOPE_AGENT);
        }
        if (layer == 1 && c == 0) late = __builtin_amdgcn_readfirstlane(*(volatile LAS unsigned*)(size_t)(ldsb + L_MISC)) == 0u;
        if (late && c == 1) {
          __builtin_amdgcn_s_setprio(3);
          if (w == 0) wait_pairs(rs, l16 & 0x1f0, FLG_KV + (8 + (blockIdx.x & 7)) * 1024 + 512, true, tag);
          BARRIER();
#pragma unroll
          for (int p = 0; p < 9; ++p) DMA(p, kvb + 2 * SLOTB, 0);
          asm volatile("s_waitcnt vmcnt(0)\n\ts_barrier" ::: "memory");
          if (strm_b) __builtin_amdgcn_s_setprio(0); else __builtin_amdgcn_s_setprio(1);
        }
        SB();
        { const int d = (sl == 0 ? SLOTB : -SLOTB) / 16; kp += d; vp += d; }
        const bool refill = (c < 2) && !(late && c == 0);
        const unsigned goff = opq(refill ? kvb + (c + 2) * SLOTB + w * 1024 : 0x80000000u);
        const unsigned dsl = opq(ldsb + sl * SLOTB + w * 1024), dsl8 = opq(w < 2 ? ldsb + sl * SLOTB + w * 1024 + 8 * 4096 : ldsb + L_DUMMY);
        UNIT(sA, sB, kY, q1, o1, vX1, o0, vY0, DM(0); DM(1), LDN(kX, vX0, vX1, 0); DM(2), DM(3); DM(4));
        UNIT(sB, sA, kX, q0, o0, vY1, o1, vY0, DM(5); DM(6), DM(7), DM(8));
        sl ^= 1;
      }
      p1[3] = pk2(e14, e15);
      o1 = mfma32(vY1, p1, o1);
    }
    __builtin_amdgcn_s_setprio(3);
    constexpr int WR = 36, WR0 = 15;
    unsigned wof[3];
    u32x4 wgr[WR];
    {
      STAGE_VARS();
      STAMP(2 + 12 * layer);
      const int tc = t < 16 ? t : (t == 16 ? 16 : 17);
      {
        const unsigned wob = opq(WO_T + ((2 * j) * 32 + 8 * w) * 1024);
#pragma unroll
        for (int i = 0; i < 8; ++i) { wof_[2 * i] = ld16<SC1>(rs, l16, wob + i * 1024); wof_[2 * i + 1] = ld16<SC1>(rs, l16, wob + (32 + i) * 1024); }
        SB();
      }
      BARRIER();
      if (layer == 0 && !strm_b && tid == 0) __hip_atomic_store((GAS u64*)(ws + FLG_G) + blockIdx.x, tag, __ATOMIC_RELAXED, __HIP_MEMORY_SCOPE_AGENT);
      STAMP0N(42);
      f32x4* dst = (f32x4*)(lds + L_A + w * 3072);
      if (kh) { dst[lane] = (f32x4){o0[0], o0[1], o0[2], o0[3]}; dst[64 + lane] = (f32x4){o0[4], o0[5], o0[6], o0[7]}; dst[128 + lane] = (f32x4){o0[8], 0.f, 0.f, 0.f}; }
      else    { dst[lane] = (f32x4){o1[0], o1[1], o1[2], o1[3]}; dst[64 + lane] = (f32x4){o1[4], o1[5], o1[6], o1[7]}; dst[128 + lane] = (f32x4){o1[8], 0.f, 0.f, 0.f}; }
      BARRIER();
      const f32x4* src = (const f32x4*)(lds + L_A + (w ^ 2) * 3072);
      const f32x4 a = src[lane], b = src[64 + lane], cc = src[128 + lane];
      float k8[9];
#pragma unroll
      for (int i = 0; i < 9; ++i) k8[i] = kh ? o1[i] : o0[i];
      float r[8] = {k8[0] + a[0], k8[1] + a[1], k8[2] + a[2], k8[3] + a[3], k8[4] + b[0], k8[5] + b[1], k8[6] + b[2], k8[7] + b[3]};
      float l = sum32(k8[8] + cc[0]);
      const bool ok = (l >= 0x1p-60f) && (l <= 0x1p60f);
      if (!__all(ok)) {
        const u32x4 qf = kh ? q1 : q0;
        f32x16 o = {};
        float m = -INFINITY;
        for (int tile = 0; tile < NRB; ++tile) {
          const u32x4 kf = ld16<SC1>(rs, l16, kvb + tile * TB);
          const u32x4 v0 = ld16<SC1>(rs, hi * (VR / 2) + tc * 16, kvb + tile * TB + 1024);
          const u32x4 v1 = ld16<SC1>(rs, hi * (VR / 2) + tc * 16, kvb + tile * TB + 1024 + VR);
          const f32x16 z = {};
          const f32x16 s = mfma32(kf, qf, z);
          float mx = s[0];
#pragma unroll
          for (int i = 1; i < 16; ++i) mx = fmaxf(mx, s[i]);
          mx = fmaxf(max32(mx), m);
          const float f = EXP2(m - mx);
          m = mx;
#pragma unroll
          for (int i = 0; i < 16; ++i) o[i] *= f;
          u32x4 p0, p1;
#pragma unroll
          for (int e = 0; e < 4; ++e) {
            p0[e] = pk2(EXP2(s[2 * e] - m), EXP2(s[2 * e + 1] - m));
            p1[e] = pk2(EXP2(s[8 + 2 * e] - m), EXP2(s[8 + 2 * e + 1] - m));
          }
          o = mfma32(v0, p0, o);
          o = mfma32(v1, p1, o);
        }
        l = sum32(o[8]);
#pragma unroll
        for (int i = 0; i < 8; ++i) r[i] = o[i];
      }
      const float il = 1.f / l;
      const u32x4 ao = {pk2(r[0] * il, r[1] * il), pk2(r[2] * il, r[3] * il), pk2(r[4] * il, r[5] * il), pk2(r[6] * il, r[7] * il)};
      const int h = 4 * j + 2 * hp + kh;
      STAMP0N(43);
      SB();
      st16<SC1>(rs, unpoison(ao), l16, AOF + ((layer * 64 + R) * 32 + h) * 1024);
      SB();
#pragma unroll
      for (int i = 0; i < 3; ++i) wof[i] = opq((w + 4 * i < nfb) ? WGU_T + (fb0 + w + 4 * i) * 32 * 1024 : 0x80000000u);
#pragma unroll
      for (int n = 0; n < WR0; ++n) wgr[n] = ld16<SC1>(rs, l16, wof[n % 3] + (n / 3) * 1024);
      SB();
      STAMP(3 + 12 * layer);
    }
    {
      STAGE_VARS();
      if (layer == 0) {
        const GAS float* u = (const GAS float*)(ws + CSGU) + fb0 * 32;
        for (int i = tid; i < nfb * 32; i += 256) {
          float s = 0.f;
#pragma unroll
          for (int p = 0; p < 8; ++p) s += __hip_atomic_load(u + p * 86 * 32 + i, __ATOMIC_RELAXED, __HIP_MEMORY_SCOPE_AGENT);
          ((float*)(lds + L_CSGU))[i] = s;
        }
      }
      const unsigned ab = opq(AOF + ((layer * 64 + R) * 32 + 8 * w) * 1024);
      u32x4 b[8];
      poll_frags<8>(rs, l16, ab, b);
      STAMP(4 + 12 * layer);
      f32x16 acc[2] = {};
#pragma unroll
      for (int i = 0; i < 8; ++i) { acc[0] = mfma32(wof_[2 * i], b[i], acc[0]); acc[1] = mfma32(wof_[2 * i + 1], b[i], acc[1]); }
      asm volatile("" : "+v"(acc[0]), "+v"(acc[1]));
      STAMP0N(32);
      red_write<2>(lds + L_B, w, lane, acc);
      BARRIER();
      STAMP0N(33);
      float v[8]; red_read8<2>(lds + L_B, w >> 1, w & 1, lane, v);
#pragma unroll
      for (int e = 0; e < 4; ++e) xr[e] = pk2(v[2 * e] + bflo(xr[e]), v[2 * e + 1] + bfhi(xr[e]));
      asm volatile("" : "+v"(xr));
      STAMP0N(34);
      SB();
      xr = unpoison(xr);
      st16<SC1>(rs, xr, l16, X1F + ((layer * 64 + R) * 32 + 4 * j + w) * 1024);
      SB();
#pragma unroll
      for (int n = WR0; n < WR; ++n) wgr[n] = ld16<SC1>(rs, l16, wof[n % 3] + (n / 3) * 1024);
      SB();
      STAMP(5 + 12 * layer);
    }
    constexpr int DR = 12;
    u32x4 wdr[2 * DR];
    unsigned db, zlast;
#define WDLD(I) do { const unsigned zf_ = (I) < 21 ? 0u : zlast; \
        wdr[2 * ((I) % DR)] = ld16<SC1>(rs, l16, zf_ | (db + (I) * 1024)); \
        wdr[2 * ((I) % DR) + 1] = ld16<SC1>(rs, l16, zf_ | (db + (86 + (I)) * 1024)); } while (0)
    {
      STAGE_VARS();
      const unsigned xb = opq(X1F + ((layer * 64 + R) * 32 + 8 * w) * 1024);
      {
        u32x4 b[8];
        poll_frags<8>(rs, l16, xb, b);
        STAMP(6 + 12 * layer);
        float s1 = 0.f, s2 = 0.f;
#pragma unroll
        for (int i = 0; i < 8; ++i) {
          *(u32x4*)(lds + L_A + (8 * w + i) * 1024 + l16) = b[i];
#pragma unroll
          for (int e = 0; e < 4; ++e) { s1 = dot2(b[i][e], ONES2, s1); s2 = dot2(b[i][e], b[i][e], s2); }
        }
        s1 = sum32(s1); s2 = sum32(s2);
        if (hi == 0) ((f32x2*)(lds + L_ST))[w * 32 + t] = (f32x2){s1, s2};
      }
      STAMP0N(35);
      BARRIER();
      STAMP0N(36);
      f32x16 acc[3] = {};
      u32x4 bnx = *(const u32x4*)(lds + L_A + l16);
#pragma unroll
      for (int ks = 0; ks < 32; ++ks) {
        const u32x4 bb = bnx;
        if (ks + 1 < 32) bnx = *(const u32x4*)(lds + L_A + (ks + 1) * 1024 + l16);
#pragma unroll
        for (int i = 0; i < 3; ++i) {
          const int n = ks * 3 + i;
          acc[i] = mfma32(wgr[n % WR], bb, acc[i]);
          if (n + WR < 96) wgr[n % WR] = ld16<SC1>(rs, l16, wof[(n + WR) % 3] + ((n + WR) / 3) * 1024);
        }
        SB();
      }
      asm volatile("" : "+v"(acc[0]), "+v"(acc[1]), "+v"(acc[2]));
      STAMP0N(37);
      float mean, rstd; ln_stats(st, t, mean, rstd);
#pragma unroll
      for (int i = 0; i < 3; ++i) {
        if (w + 4 * i < nfb) {
          const float* cs = (const float*)(lds + L_CSGU) + (w + 4 * i) * 32;
          const f32x4 g0 = *(const f32x4*)(cs + 4 * hi), g1 = *(const f32x4*)(cs + 8 + 4 * hi);
          const f32x4 u0 = *(const f32x4*)(cs + 16 + 4 * hi), u1 = *(const f32x4*)(cs + 24 + 4 * hi);
          float h[8];
#pragma unroll
          for (int e = 0; e < 8; ++e) {
            const float cg = e < 4 ? g0[e & 3] : g1[e & 3], cu = e < 4 ? u0[e & 3] : u1[e & 3];
            h[e] = geglu((acc[i][e] - mean * cg) * rstd, (acc[i][8 + e] - mean * cu) * rstd);
          }
          const u32x4 hv = {pk2(h[0], h[1]), pk2(h[2], h[3]), pk2(h[4], h[5]), pk2(h[6], h[7])};
          st16<SC1>(rs, unpoison(hv), l16, HF + ((layer * 64 + R) * 86 + fb0 + w + 4 * i) * 1024);
        }
      }
      STAMP0N(39);
      SB();
      const int k0 = w < 2 ? 22 * w : 44 + 21 * (w - 2);
      db = opq(WD_T + ((2 * j) * 86 + k0) * 1024);
      zlast = opq(w < 2 ? 0u : 0x80000000u);
#pragma unroll
      for (int i = 0; i < DR; ++i) WDLD(i);
      SB();
      STAMP(7 + 12 * layer);
    }
    {
      STAGE_VARS();
      const int k0 = w < 2 ? 22 * w : 44 + 21 * (w - 2), nk = w < 2 ? 22 : 21;
      const unsigned hb = opq(HF + ((layer * 64 + R) * 86 + k0) * 1024);
      prepoll(rs, hb, nk, lane);
      STAMP(8 + 12 * layer);
      f32x16 acc[2] = {};
      u32x4 b[11];
      load_frags<11>(rs, l16, hb, b);
      u32x4 b2[11];
#define LB2() do { _Pragma("unroll") for (int i = 0; i < 11; ++i) b2[i] = ld16<SC1>(rs, l16, (i + 11 < 21 ? 0u : zlast) | (hb + (i + 11) * 1024)); } while (0)
      LB2();
      SB();
#pragma unroll
      for (int ii = 0; ii < 11; ++ii) {
        acc[0] = mfma32(wdr[2 * (ii % DR)], b[ii], acc[0]); acc[1] = mfma32(wdr[2 * (ii % DR) + 1], b[ii], acc[1]);
        if (ii + DR < 22) WDLD(ii + DR);
        SB();
      }
      for (unsigned spins = 0; spins < SPIN_CAP && !frags_ok<11>(b2); ++spins) { asm volatile("" ::: "memory"); LB2(); }
#pragma unroll
      for (int ii = 11; ii < 22; ++ii) {
        acc[0] = mfma32(wdr[2 * (ii % DR)], b2[ii - 11], acc[0]); acc[1] = mfma32(wdr[2 * (ii % DR) + 1], b2[ii - 11], acc[1]);
        SB();
      }
      asm volatile("" : "+v"(acc[0]), "+v"(acc[1]));
      STAMP0N(40);
      red_write<2>(lds + L_B, w, lane, acc);
      BARRIER();
      STAMP0N(41);
      float v[8]; red_read8<2>(lds + L_B, w >> 1, w & 1, lane, v);
#pragma unroll
      for (int e = 0; e < 4; ++e) xr[e] = pk2(v[2 * e] + bflo(xr[e]), v[2 * e + 1] + bfhi(xr[e]));
      if (layer == 1) {
        uint16_t* p = out + (size_t)(32 * R + t) * DM + 16 * (4 * j + w) + 4 * hi;
        *(u32x2*)p = (u32x2){xr[0], xr[1]};
        *(u32x2*)(p + 8) = (u32x2){xr[2], xr[3]};
        if (blockIdx.x == 0 && tid == 0) *(GAS u64*)(ws + NONCE) = tag;
        STAMP(9 + 12 * layer);
        CSTAMP(31);
      }
    }
    if (layer == 0) {
      STAGE_VARS();
      u32x4 wqr[16], wkr[8];
      xr = unpoison(xr);
      st16<SC1>(rs, xr, l16, X2F + (R * 32 + 4 * j + w) * 1024);
      SB();
      const unsigned qb = opq(WQ_T + ((2 * j) * 32 + 8 * w) * 1024), kb = opq((role ? 0u : 0x80000000u) | (WKV_T + 8 * w * 1024));
#pragma unroll
      for (int i = 0; i < 8; ++i) {
        wqr[2 * i] = ld16<SC1>(rs, l16, qb + i * 1024);
        wqr[2 * i + 1] = ld16<SC1>(rs, l16, qb + (32 + i) * 1024);
        wkr[i] = ld16<SC1>(rs, l16, kb + i * 1024);
      }
      SB();
      STAMP(9 + 12 * layer);
      const unsigned x2b = opq(X2F + (R * 32 + 8 * w) * 1024);
      u32x4 b[8];
      poll_frags<8>(rs, l16, x2b, b);
      STAMP(10 + 12 * layer);
      f32x16 acc[3] = {};
      float s1 = 0.f, s2 = 0.f;
      if (role == 1) {
#pragma unroll
        for (int i = 0; i < 8; ++i) acc[2] = mfma32(wkr[i], b[i], acc[2]);
      } else if (role == 2) {
#pragma unroll
        for (int i = 0; i < 8; ++i) acc[2] = mfma32(b[i], wkr[i], acc[2]);
      }
#pragma unroll
      for (int i = 0; i < 8; ++i) {
        acc[0] = mfma32(wqr[2 * i], b[i], acc[0]); acc[1] = mfma32(wqr[2 * i + 1], b[i], acc[1]);
#pragma unroll
        for (int e = 0; e < 4; ++e) { s1 = dot2(b[i][e], ONES2, s1); s2 = dot2(b[i][e], b[i][e], s2); }
      }
      s1 = sum32(s1); s2 = sum32(s2);
      BARRIER();
      if (hi == 0) ((f32x2*)(lds + L_ST))[w * 32 + t] = (f32x2){s1, s2};
      red_write<3>(lds, w, lane, acc);
      BARRIER();
      if (role && w == role - 1) {
        const float* ck = (const float*)(lds + L_CSKV);
        auto cs = [&](int i) { return ck[i]; };
        auto rd = [&](int s, float* v) { red_read8<3>(lds, 2, s, role == 1 ? lane : (lane | 16), v); };
        float mean, rstd; ln_stats(st, t, mean, rstd);
        kv_finish(rs, role, rd, mean, rstd, cs, KV0 + KVL + kvpos(R) * TB, lane, lds + L_MISC + 16);
        WAITV0();
        kv_publish(ws, 1, kvpos(R), role, tag, lane);
      }
      float mean, rstd; ln_stats(st, t, mean, rstd);
      {
        float v[8];
        const float* c = (const float*)(lds + L_CSQ) + 32 * hp + 4 * hi;
        red_read8<3>(lds, hp, 0, lane, v);
        q0 = ln_pack(v, *(const f32x4*)c, *(const f32x4*)(c + 8), mean, rstd * QSCALE);
        red_read8<3>(lds, hp, 1, lane, v);
        q1 = ln_pack(v, *(const f32x4*)(c + 16), *(const f32x4*)(c + 24), mean, rstd * QSCALE);
      }
      BARRIER();
      STAMP(11 + 12 * layer);
      if (w == 0) wait_pairs(rs, l16 & 0x1f0, FLG_KV + (8 + (blockIdx.x & 7)) * 1024, true, tag);
      BARRIER();
      STAMP(12 + 12 * layer);
#pragma unroll
      for (int p = 0; p < 9; ++p) DMA(p, KV0 + KVL, 0);
    }
  }
  STAMPDUMP();
}

extern "C" void kernel_launch(void* const* d_in, const int* in_sizes, int n_in, void* d_out, int out_size, void* d_ws,
                              size_t ws_size, hipStream_t stream) {
  const uint16_t* x = (const uint16_t*)d_in[0];
  const uint16_t* wq = (const uint16_t*)d_in[1];
  const uint16_t* wk = (const uint16_t*)d_in[2];
  const uint16_t* wv = (const uint16_t*)d_in[3];
  const uint16_t* wo = (const uint16_t*)d_in[4];
  const uint16_t* wg = (const uint16_t*)d_in[6];
  const uint16_t* wu = (const uint16_t*)d_in[7];
  const uint16_t* wd = (const uint16_t*)d_in[8];
  (void)hipFuncSetAttribute((const void*)k_main, hipFuncAttributeMaxDynamicSharedMemorySize, L_END);
  k_main<<<512, 256, L_END, stream>>>(x, wq, wk, wv, wo, wg, wu, wd, (char*)d_ws, (uint16_t*)d_out);
}
```

```cpp
#include <hip/hip_runtime.h>
#include <hip/hip_bf16.h>
#include <stdint.h>

typedef float f32x2 __attribute__((ext_vector_type(2)));
typedef float f32x4 __attribute__((ext_vector_type(4)));
typedef float f32x16 __attribute__((ext_vector_type(16)));
typedef __bf16 bf16x2 __attribute__((ext_vector_type(2)));
typedef __bf16 bf16x8 __attribute__((ext_vector_type(8)));
typedef short s16x4 __attribute__((ext_vector_type(4)));
typedef unsigned u32x2 __attribute__((ext_vector_type(2)));
typedef unsigned u32x4 __attribute__((ext_vector_type(4)));
typedef unsigned long long u64;
typedef __amdgpu_buffer_rsrc_t rsrc_t;
#define DI __device__ __forceinline__
#define LAS __attribute__((address_space(3)))
#define GAS __attribute__((address_space(1)))
#define SB() __builtin_amdgcn_sched_barrier(0)
#define EXP2(x) __builtin_amdgcn_exp2f(x)

constexpr int SEQ = 2048, DM = 512, FF = 1376, NRB = 64, NFB = 86;
constexpr int VR = 576;
constexpr int TB = 1024 + 2 * VR;
constexpr int SLOTB = 16 * TB;
constexpr unsigned WQ_T = 0;
constexpr unsigned WKV_T = WQ_T + 16 * 32 * 1024;
constexpr unsigned WO_T = WKV_T + 32 * 1024;
constexpr unsigned WGU_T = WO_T + 16 * 32 * 1024;
constexpr unsigned WD_T = WGU_T + 86 * 32 * 1024;
constexpr unsigned CSGU = WD_T + 16 * 86 * 1024;
constexpr unsigned KV0 = (CSGU + 8 * 86 * 32 * 4 + 4095) / 4096 * 4096;
constexpr unsigned KVL = NRB * TB;
constexpr unsigned AOF = KV0 + 2 * KVL;
constexpr unsigned X1F = AOF + 2 * NRB * 32 * 1024;
constexpr unsigned HF = X1F + 2 * NRB * 32 * 1024;
constexpr unsigned X2F = HF + 2 * NRB * 86 * 1024;
constexpr unsigned FLG_PZ = X2F + NRB * 32 * 1024;
constexpr unsigned FLG_KV = FLG_PZ + NRB * 64;
constexpr unsigned FLG_T = FLG_KV + 2 * 8 * NRB * 16;
constexpr unsigned FLG_G = FLG_T + 512 * 8;
constexpr unsigned FLG_S = FLG_G + 256 * 8;
constexpr unsigned NONCE = FLG_S + 256 * 8;
constexpr unsigned DIAG = (NONCE + 128 + 4095) / 4096 * 4096;
constexpr unsigned WS_END = DIAG + 512 * 4 * 64 * 8;

constexpr unsigned L_A = 0, L_B = 32768;
constexpr unsigned L_TAB = 2 * SLOTB;
constexpr unsigned L_CSQ = L_TAB;
constexpr unsigned L_CSKV = L_CSQ + 256;
constexpr unsigned L_CSGU = L_CSKV + 128;
constexpr unsigned L_ST = L_CSGU + 11 * 128;
constexpr unsigned L_MISC = L_ST + 1024;
constexpr unsigned L_WKV = 65536;
constexpr unsigned L_DUMMY = 73728;
constexpr unsigned L_END = 81920;

DI f32x16 mfma32(u32x4 a, u32x4 b, f32x16 c) {
  return __builtin_amdgcn_mfma_f32_32x32x16_bf16(__builtin_bit_cast(bf16x8, a), __builtin_bit_cast(bf16x8, b), c, 0, 0, 0);
}
DI unsigned pk2(float a, float b) {
  f32x2 v = {a, b};
  return __builtin_bit_cast(unsigned, __builtin_convertvector(v, bf16x2));
}
DI float bflo(unsigned u) { return __uint_as_float(u << 16); }
DI float bfhi(unsigned u) { return __uint_as_float(u & 0xffff0000u); }
DI float sum32(float v) {
  auto r = __builtin_amdgcn_permlane32_swap(__float_as_uint(v), __float_as_uint(v), false, false);
  return __uint_as_float(r[0]) + __uint_as_float(r[1]);
}
DI float max32(float v) {
  auto r = __builtin_amdgcn_permlane32_swap(__float_as_uint(v), __float_as_uint(v), false, false);
  return fmaxf(__uint_as_float(r[0]), __uint_as_float(r[1]));
}
DI rsrc_t make_rsrc(const void* p, unsigned bytes) { return __builtin_amdgcn_make_buffer_rsrc((void*)p, 0, bytes, 0x00020000); }
constexpr int SC1 = 16;
template <int AUX> DI u32x4 ld16(rsrc_t r, unsigned voff, unsigned soff) { return __builtin_amdgcn_raw_buffer_load_b128(r, voff, soff, AUX); }
template <int AUX> DI void st16(rsrc_t r, u32x4 v, unsigned voff, unsigned soff) {
  __builtin_amdgcn_raw_buffer_store_b128(v, r, voff, soff, AUX);
  asm volatile("s_nop 1" :: "v"(v));
}
DI u32x2 ld8(rsrc_t r, unsigned voff, unsigned soff) { return __builtin_amdgcn_raw_buffer_load_b64(r, voff, soff, 0); }
template <int AUX> DI void glds16(rsrc_t r, unsigned voff, unsigned soff, unsigned ldsaddr) {
  __builtin_amdgcn_raw_ptr_buffer_load_lds(r, (LAS void*)(size_t)ldsaddr, 16, voff, soff, 0, AUX);
}
DI float dot2(unsigned a, unsigned b, float c) {
  return __builtin_amdgcn_fdot2_f32_bf16(__builtin_bit_cast(bf16x2, a), __builtin_bit_cast(bf16x2, b), c, false);
}
constexpr unsigned ONES2 = 0x3f803f80u;
DI unsigned opq(unsigned x) { x = __builtin_amdgcn_readfirstlane(x); asm volatile("" : "+s"(x)); return x; }
DI u32x2 tr8(unsigned ldsaddr) {
  return __builtin_bit_cast(u32x2, __builtin_amdgcn_ds_read_tr16_b64_v4i16((LAS s16x4*)(size_t)ldsaddr));
}
DI u32x4 tr_frag(unsigned a, unsigned row_stride) {
  const u32x2 lo = tr8(a), hi = tr8(a + 8 * row_stride);
  return (u32x4){lo[0], lo[1], hi[0], hi[1]};
}
DI u32x4 tr_frag_nat(unsigned a, unsigned row_stride) {
  const u32x2 lo = tr8(a), hi = tr8(a + 4 * row_stride);
  return (u32x4){lo[0], lo[1], hi[0], hi[1]};
}
DI float frag_sum(u32x4 f, float s) {
#pragma unroll
  for (int e = 0; e < 4; ++e) s = dot2(f[e], ONES2, s);
  return s;
}
#define WAITV0() asm volatile("s_waitcnt vmcnt(0)" ::: "memory")
#define BARRIER() asm volatile("s_waitcnt lgkmcnt(0)\n\ts_barrier" ::: "memory")

#define PA_ATT0 2
#define PA_ATT1A 0
#define PA_ATT1B 2
#define PA_CH 3
#define PB_ATT 1
#define PB_CH 3
#define JOB_PRIO 1
#define GATE_CH 2
#define XSPLIT 2
DI void decode_block(int b, int& R, int& j) {
  const int x = b & 7, r = b >> 3, strm = r >> 5, rr = r & 31;
  constexpr int MPX = 8 / XSPLIT;
  const int h = x % XSPLIT, p = x / XSPLIT;
  j = MPX * h + rr % MPX;
  const int rl = rr / MPX;
  R = 8 * XSPLIT * p + 8 * (rl >> 2) + 4 * strm + (rl & 3);
}
DI int kvpos(int R) { return ((R & 4) << 3) + ((R >> 3) << 2) + (R & 3); }

template <int NT> DI void red_write(char* lds, int w, int lane, const f32x16* acc) {
#pragma unroll
  for (int t = 0; t < NT; ++t)
#pragma unroll
    for (int q = 0; q < 4; ++q)
      *(f32x4*)(lds + (((w * NT + t) * 4 + q) * 64 + lane) * 16) = (f32x4){acc[t][4 * q], acc[t][4 * q + 1], acc[t][4 * q + 2], acc[t][4 * q + 3]};
}
template <int NT> DI void red_read8(const char* lds, int t, int s, int lane, float* v) {
#pragma unroll
  for (int qq = 0; qq < 2; ++qq) {
    f32x4 a = *(const f32x4*)(lds + (((0 * NT + t) * 4 + 2 * s + qq) * 64 + lane) * 16);
#pragma unroll
    for (int w = 1; w < 4; ++w) a += *(const f32x4*)(lds + (((w * NT + t) * 4 + 2 * s + qq) * 64 + lane) * 16);
    v[4 * qq] = a[0]; v[4 * qq + 1] = a[1]; v[4 * qq + 2] = a[2]; v[4 * qq + 3] = a[3];
  }
}
template <int NT, int WS> DI void red_write_s(char* lds, int w, int lane, const f32x16* acc) {
#pragma unroll
  for (int t = 0; t < NT; ++t)
#pragma unroll
    for (int q = 0; q < 4; ++q)
      *(f32x4*)(lds + w * WS + ((t * 4 + q) * 64 + lane) * 16) = (f32x4){acc[t][4 * q], acc[t][4 * q + 1], acc[t][4 * q + 2], acc[t][4 * q + 3]};
}
template <int WS> DI void red_read8_s(const char* lds, int t, int s, int lane, float* v) {
#pragma unroll
  for (int qq = 0; qq < 2; ++qq) {
    f32x4 a = *(const f32x4*)(lds + ((t * 4 + 2 * s + qq) * 64 + lane) * 16);
#pragma unroll
    for (int w = 1; w < 4; ++w) a += *(const f32x4*)(lds + w * WS + ((t * 4 + 2 * s + qq) * 64 + lane) * 16);
    v[4 * qq] = a[0]; v[4 * qq + 1] = a[1]; v[4 * qq + 2] = a[2]; v[4 * qq + 3] = a[3];
  }
}
constexpr unsigned POISON = 0xffffffffu;
DI u32x4 poison4() { return (u32x4){POISON, POISON, POISON, POISON}; }
DI u32x4 pack8(const float* h) { return (u32x4){pk2(h[0], h[1]), pk2(h[2], h[3]), pk2(h[4], h[5]), pk2(h[6], h[7])}; }
DI u32x4 unpoison(u32x4 v) { return (u32x4){min(v[0], POISON - 1), min(v[1], POISON - 1), min(v[2], POISON - 1), min(v[3], POISON - 1)}; }
template <int N> DI bool frags_ok(const u32x4* b) {
  unsigned m = 0;
#pragma unroll
  for (int i = 0; i < N; ++i) m = max(max(max(m, b[i][0]), max(b[i][1], b[i][2])), b[i][3]);
  return __all(m != POISON);
}
template <int SS> DI void ln_stats_s(const f32x2* st, int tok, float& mean, float& rstd) {
  const f32x2 a = st[tok] + st[SS + tok] + st[2 * SS + tok] + st[3 * SS + tok];
  mean = a[0] * (1.f / 512.f);
  rstd = __builtin_amdgcn_rsqf(fmaxf(a[1] * (1.f / 512.f) - mean * mean, 0.f) + 1e-5f);
}
DI void ln_stats(const f32x2* st, int tok, float& mean, float& rstd) {
  const f32x2 a = st[tok] + st[32 + tok] + st[64 + tok] + st[96 + tok];
  mean = a[0] * (1.f / 512.f);
  rstd = __builtin_amdgcn_rsqf(fmaxf(a[1] * (1.f / 512.f) - mean * mean, 0.f) + 1e-5f);
}
DI u32x4 ln_pack(const float* v, f32x4 c0, f32x4 c1, float mean, float sc) {
  return (u32x4){pk2((v[0] - mean * c0[0]) * sc, (v[1] - mean * c0[1]) * sc), pk2((v[2] - mean * c0[2]) * sc, (v[3] - mean * c0[3]) * sc),
                 pk2((v[4] - mean * c1[0]) * sc, (v[5] - mean * c1[1]) * sc), pk2((v[6] - mean * c1[2]) * sc, (v[7] - mean * c1[3]) * sc)};
}
constexpr float QSCALE = 0.25f * 1.4426950408889634f;

template <int N> DI void set_tag(char* ws, unsigned off, u64 tag, int lane) {
  asm volatile("s_waitcnt vmcnt(%0)" :: "n"(N) : "memory");
  if (lane == 0) __hip_atomic_store((GAS u64*)(ws + off), tag, __ATOMIC_RELAXED, __HIP_MEMORY_SCOPE_AGENT);
}
DI void put_tag(GAS u64* p, u64 tag) {
  asm volatile("" : "+s"(tag));
  __hip_atomic_store(p, tag, __ATOMIC_RELAXED, __HIP_MEMORY_SCOPE_AGENT);
}
constexpr unsigned SPIN_CAP = 1u << 17;
DI void wait_tags(const char* ws, unsigned off, bool active, u64 tag) {
  const GAS u64* p = (const GAS u64*)(ws + off);
  for (unsigned spins = 0; spins < SPIN_CAP; ++spins) {
    const u64 v = active ? __hip_atomic_load(p, __ATOMIC_RELAXED, __HIP_MEMORY_SCOPE_AGENT) : tag;
    if (__all(v == tag)) return;
    __builtin_amdgcn_s_sleep(4);
  }
}
DI bool pair_ok(u32x4 v, u64 tag) { return v[0] == (unsigned)tag && v[1] == (unsigned)(tag >> 32) && v[2] == (unsigned)tag && v[3] == (unsigned)(tag >> 32); }
DI void wait_pairs(rsrc_t rs, unsigned voff, unsigned soff, bool active, u64 tag) {
  for (unsigned spins = 0; spins < SPIN_CAP; ++spins) {
    asm volatile("" ::: "memory");
    const u32x4 v = ld16<SC1>(rs, voff, soff);
    if (__all(!active || pair_ok(v, tag))) return;
    __builtin_amdgcn_s_sleep(4);
  }
}
DI void kv_publish(char* ws, int layer, int pos, int role, u64 tag, int lane) {
  if (lane < 8) put_tag((GAS u64*)(ws + FLG_KV) + ((layer * 8 + lane) * 64 + pos) * 2 + (role - 1), tag);
}
DI float geglu(float g, float u) {
  const float z = g * __builtin_fmaf(g * g, -0.044715f * 2.f * 0.7978845608028654f * 1.4426950408889634f, -2.f * 0.7978845608028654f * 1.4426950408889634f);
  return g * u * __builtin_amdgcn_rcpf(1.f + EXP2(z));
}

DI void prepoll(rsrc_t rs, unsigned soff, int n, int lane) {
  const unsigned po = min(lane, n - 1) * 1024 + 1020;
  for (unsigned spins = 0; spins < SPIN_CAP; ++spins) {
    asm volatile("" ::: "memory");
    const unsigned v = __builtin_amdgcn_raw_buffer_load_b32(rs, po, soff, SC1);
    if (__all(v != POISON)) return;
    __builtin_amdgcn_s_sleep(1);
  }
}
template <int N> DI void poll_frags(rsrc_t rs, unsigned voff, unsigned soff, u32x4* b) {
  prepoll(rs, soff, N, voff >> 4);
  for (unsigned spins = 0; spins < SPIN_CAP; ++spins) {
    asm volatile("" ::: "memory");
#pragma unroll
    for (int i = 0; i < N; ++i) b[i] = ld16<SC1>(rs, voff, soff + i * 1024);
    if (frags_ok<N>(b)) return;
    __builtin_amdgcn_s_sleep(2);
  }
}
template <int N> DI void load_frags(rsrc_t rs, unsigned voff, unsigned soff, u32x4* b) {
  for (unsigned spins = 0; spins < SPIN_CAP; ++spins) {
    asm volatile("" ::: "memory");
#pragma unroll
    for (int i = 0; i < N; ++i) b[i] = ld16<SC1>(rs, voff, soff + i * 1024);
    if (frags_ok<N>(b)) return;
    __builtin_amdgcn_s_sleep(2);
  }
}

template <class RD, class CS>
DI void kv_finish(rsrc_t rs, int role, RD rd, float mean, float rstd, CS cs, unsigned kvbase, int lane, char* scr) {
  const int hi = lane >> 5, t = lane & 31;
  if (role == 1) {
    float v[8]; rd(0, v);
    const f32x4 c0 = {cs(4 * hi), cs(4 * hi + 1), cs(4 * hi + 2), cs(4 * hi + 3)}, c1 = {cs(8 + 4 * hi), cs(9 + 4 * hi), cs(10 + 4 * hi), cs(11 + 4 * hi)};
    st16<SC1>(rs, ln_pack(v, c0, c1, mean, rstd), lane * 16, kvbase);
  } else {
    float* sm = (float*)scr;
    if (hi == 0) { sm[t] = rstd; sm[32 + t] = mean * rstd; }
    asm volatile("s_waitcnt lgkmcnt(0)" ::: "memory");
    const float c = cs(t & 15);
#pragma unroll
    for (int s = 0; s < 2; ++s) {
      float v[8]; rd(s, v);
      const f32x4 r0 = *(const f32x4*)(sm + 16 * s + 4 * hi), r1 = *(const f32x4*)(sm + 16 * s + 8 + 4 * hi);
      const f32x4 m0 = *(const f32x4*)(sm + 32 + 16 * s + 4 * hi), m1 = *(const f32x4*)(sm + 32 + 16 * s + 8 + 4 * hi);
      u32x4 o = {pk2(v[0] * r0[0] - m0[0] * c, v[1] * r0[1] - m0[1] * c), pk2(v[2] * r0[2] - m0[2] * c, v[3] * r0[3] - m0[3] * c),
                 pk2(v[4] * r1[0] - m1[0] * c, v[5] * r1[1] - m1[1] * c), pk2(v[6] * r1[2] - m1[2] * c, v[7] * r1[3] - m1[3] * c)};
      if (t >= 16) { const unsigned k = t == 16 ? ONES2 : 0u; o = (u32x4){k, k, k, k}; }
      if (t < 18) st16<SC1>(rs, o, hi * (VR / 2) + t * 16, kvbase + 1024 + s * VR);
    }
  }
}
DI void stats_of(f32x2 a, float& mean, float& rstd) {
  mean = a[0] * (1.f / 512.f);
  rstd = __builtin_amdgcn_rsqf(fmaxf(a[1] * (1.f / 512.f) - mean * mean, 0.f) + 1e-5f);
}

#define STAMP(i) do { } while (0)
#define CSTAMP(i) do { } while (0)
#define STAMP0N(i) do { } while (0)
#define STAMPV(i, v) do { } while (0)
#define STAMPG(i) do { if ((threadIdx.x & 63) == 0) ((unsigned long long*)(ws + DIAG))[blockIdx.x * 256 + (threadIdx.x >> 6) * 64 + (i)] = __builtin_amdgcn_s_memrealtime(); } while (0)
#define CSTAMPG(i) do { if ((threadIdx.x & 63) == 0) ((unsigned long long*)(ws + DIAG))[blockIdx.x * 256 + (threadIdx.x >> 6) * 64 + (i)] = __builtin_amdgcn_s_memtime(); } while (0)
#define STAMPINIT() do { } while (0)
#define STAMPG(i) do { } while (0)
#define CSTAMPG(i) do { } while (0)
#define STAMPDUMP() do { } while (0)

#define STAGE_VARS()                                                                                              \
  int tid = threadIdx.x; asm volatile("" : "+v"(tid));                                                            \
  int R = R_, j = j_, w = w_; asm volatile("" : "+s"(R), "+s"(j), "+s"(w));                                       \
  const int lane = tid & 63, hi = lane >> 5, t = lane & 31, hp = w & 1, kh = w >> 1;                              \
  const unsigned l16 = lane * 16;                                                                                 \
  const int nfb = j < 6 ? 11 : 10, fb0 = j < 6 ? 11 * j : 66 + 10 * (j - 6);                                      \
  const int role = j == (R & 7) ? 1 : (j == ((R + 1) & 7) ? 2 : 0);      \
  (void)hi; (void)t; (void)hp; (void)kh; (void)l16; (void)nfb; (void)fb0; (void)R; (void)role;

__global__ __launch_bounds__(256, 2) void k_main(const uint16_t* __restrict__ x, const uint16_t* __restrict__ wq,
                                                 const uint16_t* __restrict__ wk, const uint16_t* __restrict__ wv,
                                                 const uint16_t* __restrict__ wo, const uint16_t* __restrict__ wg,
                                                 const uint16_t* __restrict__ wu, const uint16_t* __restrict__ wd,
                                                 char* __restrict__ ws, uint16_t* __restrict__ out) {
  extern __shared__ __attribute__((aligned(16))) char lds[];
  int R_, j_; decode_block(blockIdx.x, R_, j_);
  const int w_ = __builtin_amdgcn_readfirstlane(threadIdx.x >> 6);
  const rsrc_t rs = make_rsrc(ws, WS_END);
  const unsigned ldsb = (unsigned)(size_t)(LAS char*)lds;
  const bool strm_b = (R_ & 4) != 0;
  const f32x2* st = (const f32x2*)(lds + L_ST);
  u32x4 q0, q1;
  u32x4 xr;
  u64 tag;
#define DMA(P, GOFF, SLOT) do { if ((P) < 8 || w < 2) glds16<SC1>(rs, l16, (GOFF) + w * 1024 + (P) * 4096, ldsb + (SLOT) * SLOTB + w * 1024 + (P) * 4096); } while (0)
  {
    STAGE_VARS();
    STAMPG(0);
    CSTAMPG(30);
    if (role) __builtin_amdgcn_s_setprio(3);
    const u32x2 nv = *(const GAS u32x2*)(ws + NONCE);
    if (role) {
      const rsrc_t rk = make_rsrc(role == 1 ? wk : wv, DM * 16 * 2);
#pragma unroll
      for (int n = 0; n < 4; ++n) glds16<0>(rk, l16, (128 * w + 32 * n) * 32, ldsb + L_WKV + (128 * w + 32 * n) * 32);
    }
    {
      const rsrc_t rx = make_rsrc(x, SEQ * DM * 2);
#pragma unroll
      for (int n = 0; n < 8; ++n) glds16<0>(rx, (lane >> 4) * 1024 + (lane & 15) * 16, R * 32768 + n * 4096 + w * 256, ldsb + w * 16384 + n * 1040);
    }
    WAITV0();
    u32x4 xb[8];
#pragma unroll
    for (int i = 0; i < 8; ++i) xb[i] = *(const u32x4*)(lds + w * 16384 + (t >> 2) * 1040 + (t & 3) * 256 + i * 32 + hi * 16);
    asm volatile("s_waitcnt lgkmcnt(0)" : "+v"(xb[0]), "+v"(xb[1]), "+v"(xb[2]), "+v"(xb[3]), "+v"(xb[4]), "+v"(xb[5]), "+v"(xb[6]), "+v"(xb[7]) :: "memory");
    {
      const rsrc_t rq = make_rsrc(wq, DM * DM * 2);
#pragma unroll
      for (int n = 0; n < 16; ++n)
        glds16<0>(rq, (lane >> 3) * 1024 + (lane & 7) * 16, (128 * w + 8 * n) * 1024 + j * 128, ldsb + (128 * w + 8 * n) * 128);
    }
    SB();
    tag = (((u64)__builtin_amdgcn_readfirstlane(nv[1]) << 32) | (u64)__builtin_amdgcn_readfirstlane(nv[0])) + 1;
    STAMPG(25);
    const unsigned trl = (8 * hi + ((lane & 15) >> 2)) * 128 + (((lane >> 4) & 1) * 16 + (lane & 3) * 4) * 2;
    const unsigned trk = (8 * hi + ((lane & 15) >> 2)) * 32 + (lane & 3) * 8;
    const unsigned trlp = trl - 4 * hi * 128, trkp = trk - 4 * hi * 32;
    const int bq = strm_b ? kvpos(R) - 32 : -1;
    char* const qw = lds + L_WKV + w * 4096;
    float s1 = 0.f, s2 = 0.f;
#pragma unroll
    for (int i = 0; i < 8; ++i)
#pragma unroll
      for (int e = 0; e < 4; ++e) { s1 = dot2(xb[i][e], ONES2, s1); s2 = dot2(xb[i][e], xb[i][e], s2); }
    s1 = sum32(s1); s2 = sum32(s2);
    if (role) {
      f32x16 akv = {};
      float cskv = 0.f;
      u32x4 kf[8];
#pragma unroll
      for (int i = 0; i < 8; ++i) { kf[i] = tr_frag_nat(ldsb + L_WKV + (8 * w + i) * 16 * 32 + trk, 32); cskv = frag_sum(kf[i], cskv); }
      if (role == 1) {
#pragma unroll
        for (int i = 0; i < 8; ++i) akv = mfma32(kf[i], xb[i], akv);
      } else {
#pragma unroll
        for (int i = 0; i < 8; ++i) akv = mfma32(xb[i], kf[i], akv);
      }
      if (R == 4) {
#pragma unroll
        for (int i = 0; i < 8; ++i) {
          const u32x4 ap = tr_frag(ldsb + L_WKV + (8 * w + i) * 16 * 32 + trkp, 32);
          if (((lane >> 4) & 1) == role - 1) st16<SC1>(rs, ap, l16, WKV_T + (8 * w + i) * 1024);
        }
      }
      cskv = sum32(cskv);
      asm volatile("s_waitcnt lgkmcnt(0)" ::: "memory");
      if (role == 1) {
        *(f32x4*)(qw + l16) = (f32x4){akv[0], akv[1], akv[2], akv[3]};
        *(f32x4*)(qw + 1024 + l16) = (f32x4){akv[4], akv[5], akv[6], akv[7]};
      } else if (t < 16) {
#pragma unroll
        for (int q = 0; q < 4; ++q) *(f32x4*)(qw + ((q * 2 + hi) * 16 + t) * 16) = (f32x4){akv[4 * q], akv[4 * q + 1], akv[4 * q + 2], akv[4 * q + 3]};
      }
      if (hi == 0) ((float*)(qw + 2304))[t] = cskv;
    }
    if (hi == 0) ((f32x2*)(qw + 2048))[t] = (f32x2){s1, s2};
    BARRIER();
    auto stat = [&](int tok, float& mean, float& rstd) {
      const char* p = lds + L_WKV + 2048 + tok * 8;
      stats_of(*(const f32x2*)p + *(const f32x2*)(p + 4096) + *(const f32x2*)(p + 8192) + *(const f32x2*)(p + 12288), mean, rstd);
    };
    auto cskv4 = [&](int i) { const float* p = (const float*)(lds + L_WKV + 2304) + i; return p[0] + p[1024] + p[2048] + p[3072]; };
    if (role && w == role - 1) {
      auto rd = [&](int s, float* v) {
#pragma unroll
        for (int qq = 0; qq < 2; ++qq) {
          const char* p = lds + L_WKV + (role == 1 ? qq * 1024 + l16 : (((2 * s + qq) * 2 + hi) * 16 + (t & 15)) * 16);
          const f32x4 a = *(const f32x4*)p + *(const f32x4*)(p + 4096) + *(const f32x4*)(p + 8192) + *(const f32x4*)(p + 12288);
          v[4 * qq] = a[0]; v[4 * qq + 1] = a[1]; v[4 * qq + 2] = a[2]; v[4 * qq + 3] = a[3];
        }
      };
      float mean, rstd; stat(t, mean, rstd);
      kv_finish(rs, role, rd, mean, rstd, cskv4, KV0 + kvpos(R) * TB, lane, qw + 3072);
      WAITV0();
      kv_publish(ws, 0, kvpos(R), role, tag, lane);
    }
    STAMPG(26);
    WAITV0();
    {
      const rsrc_t rx = make_rsrc(x, SEQ * DM * 2);
      const u32x2 a = ld8(rx, t * 1024 + hi * 8, R * 32768 + (4 * j + w) * 32), b = ld8(rx, t * 1024 + hi * 8 + 16, R * 32768 + (4 * j + w) * 32);
      xr = (u32x4){a[0], a[1], b[0], b[1]};
    }
    f32x16 acc[2] = {};
    float csq0 = 0.f, csq1 = 0.f;
#pragma unroll
    for (int i = 0; i < 8; ++i) {
      const int ks = 8 * w + i;
      const u32x4 a0 = tr_frag_nat(ldsb + ks * 16 * 128 + trl, 128), a1 = tr_frag_nat(ldsb + ks * 16 * 128 + 64 + trl, 128);
      acc[0] = mfma32(a0, xb[i], acc[0]);
      acc[1] = mfma32(a1, xb[i], acc[1]);
      csq0 = frag_sum(a0, csq0); csq1 = frag_sum(a1, csq1);
      if (bq == ks) {
        st16<SC1>(rs, tr_frag(ldsb + ks * 16 * 128 + trlp, 128), l16, WQ_T + ((2 * j) * 32 + ks) * 1024);
        st16<SC1>(rs, tr_frag(ldsb + ks * 16 * 128 + 64 + trlp, 128), l16, WQ_T + ((2 * j + 1) * 32 + ks) * 1024);
      }
    }
    csq0 = sum32(csq0); csq1 = sum32(csq1);
    asm volatile("s_waitcnt lgkmcnt(0)" ::: "memory");
    red_write_s<2, 16384>(lds, w, lane, acc);
    if (hi == 0) { ((float*)(lds + w * 16384 + 8192))[t] = csq0; ((float*)(lds + w * 16384 + 8192))[32 + t] = csq1; }
    BARRIER();
    {
      float mean, rstd; stat(t, mean, rstd);
      f32x4 c0 = {}, c1 = {}, c2 = {}, c3 = {};
#pragma unroll
      for (int p = 0; p < 4; ++p) {
        const float* c = (const float*)(lds + p * 16384 + 8192) + 32 * hp + 4 * hi;
        c0 += *(const f32x4*)c; c1 += *(const f32x4*)(c + 8); c2 += *(const f32x4*)(c + 16); c3 += *(const f32x4*)(c + 24);
      }
      float v[8];
      red_read8_s<16384>(lds, hp, 0, lane, v); q0 = ln_pack(v, c0, c1, mean, rstd * QSCALE);
      red_read8_s<16384>(lds, hp, 1, lane, v); q1 = ln_pack(v, c2, c3, mean, rstd * QSCALE);
    }
    float tabv = 0.f;
    if (tid < 64) { const float* c = (const float*)(lds + 8192) + tid; tabv = c[0] + c[4096] + c[8192] + c[12288]; }
    else if (tid < 96 && role) tabv = cskv4(tid - 64);
    BARRIER();
    if (tid < 96) ((float*)(lds + L_TAB))[tid] = tabv;
    STAMPG(27);
    __builtin_amdgcn_s_setprio(0);
    STAMPG(28);
    if (w == 0) wait_pairs(rs, l16, FLG_KV + (blockIdx.x & 7) * 1024, true, tag);
    BARRIER();
    STAMPG(29);
    if (strm_b) {
      if (w == 0) wait_tags(ws, FLG_S + (blockIdx.x - 256) * 8, lane == 0, tag);
      BARRIER();
      __builtin_amdgcn_s_setprio(JOB_PRIO);
      const int nj = blockIdx.x - 256 < 40 ? 2 : 1;
#pragma unroll
      for (int k = 0; k < 2; ++k) {
        if (k < nj) {
          const int job = blockIdx.x - 256 + 256 * k;
          const unsigned pan = ldsb + k * 16384;
          if (job < 120) {
            const bool o = job < 32;
            const int jj = o ? job : job - 32, kb = jj >> 3, cp = jj & 7;
            const rsrc_t r = make_rsrc(o ? wo : wd, o ? DM * DM * 2 : FF * DM * 2);
#pragma unroll
            for (int n = 0; n < 4; ++n)
              glds16<0>(r, (lane >> 3) * 1024 + (lane & 7) * 16, (128 * kb + 32 * w + 8 * n) * 1024 + cp * 128, pan + (32 * w + 8 * n) * 128);
          } else {
            const int jj = job - 120, kb = jj & 7, fb = min(4 * (jj >> 3) + w, NFB - 1);
            const rsrc_t rg = make_rsrc(wg, DM * FF * 2), ru = make_rsrc(wu, DM * FF * 2);
#pragma unroll
            for (int n = 0; n < 2; ++n) {
              glds16<0>(rg, (lane >> 1) * (FF * 2) + (lane & 1) * 16, (64 * kb + 32 * n) * (FF * 2) + fb * 32, pan + w * 4096 + n * 1024);
              glds16<0>(ru, (lane >> 1) * (FF * 2) + (lane & 1) * 16, (64 * kb + 32 * n) * (FF * 2) + fb * 32, pan + w * 4096 + 2048 + n * 1024);
            }
          }
        }
      }
      WAITV0();
#pragma unroll
      for (int k = 0; k < 2; ++k) {
        if (k < nj) {
          const int job = blockIdx.x - 256 + 256 * k;
          const unsigned pan = ldsb + k * 16384;
          if (job < 120) {
            const bool o = job < 32;
            const int jj = o ? job : job - 32, kb = jj >> 3, cp = jj & 7;
#pragma unroll
            for (int i = 0; i < 2; ++i) {
              const int ks = 8 * kb + 2 * w + i;
              if (o || ks < NFB) {
#pragma unroll
                for (int c = 0; c < 2; ++c) {
                  const u32x4 a = tr_frag(pan + (32 * w + 16 * i) * 128 + c * 64 + trlp, 128);
                  st16<SC1>(rs, a, l16, o ? WO_T + ((2 * cp + c) * 32 + ks) * 1024 : WD_T + ((2 * cp + c) * 86 + ks) * 1024);
                }
              }
            }
          } else {
            const int jj = job - 120, kb = jj & 7, fb = 4 * (jj >> 3) + w;
            if (fb < NFB) {
              const unsigned tg = pan + w * 4096 + ((lane >> 4) & 1) * 2048 + trkp;
              float cs = 0.f;
#pragma unroll
              for (int i = 0; i < 4; ++i) {
                const u32x4 a = tr_frag(tg + i * 16 * 32, 32);
                st16<SC1>(rs, a, l16, WGU_T + (fb * 32 + 4 * kb + i) * 1024);
                cs = frag_sum(a, cs);
              }
              cs = sum32(cs);
              if (hi == 0) __hip_atomic_store((GAS float*)(ws + CSGU) + (kb * 86 + fb) * 32 + t, cs, __ATOMIC_RELAXED, __HIP_MEMORY_SCOPE_AGENT);
            }
          }
        }
      }
      WAITV0();
      BARRIER();
      if (tid == 0) put_tag((GAS u64*)(ws + FLG_T) + blockIdx.x, tag);
    }
#pragma unroll
    for (int p = 0; p < 9; ++p) DMA(p, KV0, 0);
    if (strm_b) {
      if (w == 0) wait_tags(ws, FLG_G + (blockIdx.x - 256) * 8, lane == 0, tag);
    }
  }

  STAMPINIT();
#pragma unroll 1
  for (int layer = 0; layer < 2; ++layer) {
    const unsigned kvb = KV0 + layer * KVL;
    constexpr int RING = 36, NPRE = 28;
    u32x4 W[RING];
    unsigned wob, wf0, wf1, wf2, db, zlast;
#define WOFF(S) ((S) < 16 ? wob + (((S) & 1) * 32 + ((S) >> 1)) * 1024 \
               : (S) < 48 ? wf0 + ((S) - 16) * 1024 : (S) < 80 ? wf1 + ((S) - 48) * 1024 : (S) < 112 ? wf2 + ((S) - 80) * 1024 \
               : ((((S) - 112) >> 1) < 21 ? 0u : zlast) | (db + ((((S) - 112) & 1) * 86 + (((S) - 112) >> 1)) * 1024))
#define WLD(S) do { if ((S) < 156) W[(S) % RING] = ld16<SC1>(rs, l16, WOFF(S)); } while (0)
    f32x16 o0 = {}, o1 = {};
    {
      STAGE_VARS();
      if (strm_b) __builtin_amdgcn_s_setprio(PB_ATT); else if (layer == 0) __builtin_amdgcn_s_setprio(PA_ATT0); else __builtin_amdgcn_s_setprio(PA_ATT1A);
      asm volatile("s_waitcnt vmcnt(0) lgkmcnt(0)\n\ts_barrier" ::: "memory");
      STAMP(1 + 12 * layer);
      if (layer == 0 && !strm_b && tid == 0) put_tag((GAS u64*)(ws + FLG_S) + blockIdx.x, tag);
#pragma unroll
      for (int p = 0; p < 9; ++p) DMA(p, kvb + SLOTB, 1);
      if (layer == 0) {
        const u32x4 pz = poison4();
#pragma unroll
        for (int l = 0; l < 2; ++l) {
          st16<SC1>(rs, pz, l16, AOF + ((l * 64 + R) * 32 + 4 * j + 2 * hp + kh) * 1024);
          st16<SC1>(rs, pz, l16, X1F + ((l * 64 + R) * 32 + 4 * j + w) * 1024);
#pragma unroll
          for (int i = 0; i < 3; ++i) st16<SC1>(rs, pz, l16, (w + 4 * i < nfb ? 0u : 0x80000000u) | (HF + ((l * 64 + R) * 86 + fb0 + w + 4 * i) * 1024));
        }
        st16<SC1>(rs, pz, l16, X2F + (R * 32 + 4 * j + w) * 1024);
      }
      SB();
      const int tc = t < 16 ? t : (t == 16 ? 16 : 17);
      const LAS u32x4* kp = (const LAS u32x4*)(size_t)(ldsb + kh * 8 * TB + l16);
      const LAS u32x4* vp = (const LAS u32x4*)(size_t)(ldsb + kh * 8 * TB + 1024 + hi * (VR / 2) + tc * 16);
      const f32x16 z = {};
      f32x16 sA, sB;
      u32x4 kX, vX0, vX1, kY, vY0, vY1, p0, p1;
      kX = kp[0]; vX0 = vp[0]; vX1 = vp[VR / 16];
      sA = mfma32(kX, q0, z);
      vY1 = vX1;
      p1 = (u32x4){0, 0, 0, 0};
      float e14 = 0.f, e15 = 0.f;
#define UNIT(SC, SN, KN, QN, OP, VP1, OC, VC0, A1, A2, A3)                                                               \
  do {                                                                                                                   \
    SN = mfma32(KN, QN, z); SB();                                                                                        \
    A1;                                                                                                                  \
    float e0 = EXP2(SC[0]), e1 = EXP2(SC[1]); SB();                                                                      \
    p1[3] = pk2(e14, e15); SB();                                                                                         \
    float e2 = EXP2(SC[2]), e3 = EXP2(SC[3]); SB();                                                                      \
    OP = mfma32(VP1, p1, OP); SB();                                                                                      \
    A2;                                                                                                                  \
    p0[0] = pk2(e0, e1); SB();                                                                                           \
    float e4 = EXP2(SC[4]), e5 = EXP2(SC[5]); SB();                                                                      \
    p0[1] = pk2(e2, e3); SB();                                                                                           \
    float e6 = EXP2(SC[6]), e7 = EXP2(SC[7]); SB();                                                                      \
    p0[2] = pk2(e4, e5); SB();                                                                                           \
    float e8 = EXP2(SC[8]), e9 = EXP2(SC[9]); SB();                                                                      \
    p0[3] = pk2(e6, e7); SB();                                                                                           \
    float e10 = EXP2(SC[10]), e11 = EXP2(SC[11]); SB();                                                                  \
    OC = mfma32(VC0, p0, OC); SB();                                                                                      \
    A3;                                                                                                                  \
    p1[0] = pk2(e8, e9); SB();                                                                                           \
    float e12 = EXP2(SC[12]), e13 = EXP2(SC[13]); SB();                                                                  \
    p1[1] = pk2(e10, e11); SB();                                                                                         \
    e14 = EXP2(SC[14]); e15 = EXP2(SC[15]); SB();                                                                        \
    p1[2] = pk2(e12, e13); SB();                                                                                         \
  } while (0)
#define LDN(KN, VN0, VN1, NOFF) do { KN = kp[(NOFF) / 16]; VN0 = vp[(NOFF) / 16]; VN1 = vp[((NOFF) + VR) / 16]; SB(); } while (0)
#define TILE(KC, VC0, VC1, KN, VN0, VN1, NOFF, X0, X1)                          \
    UNIT(sA, sB, KC, q1, o1, VN1, o0, VC0, X0, LDN(KN, VN0, VN1, NOFF), );      \
    UNIT(sB, sA, KN, q0, o0, VC1, o1, VC0, X1, , );
#define PINP() asm volatile("" : "+v"(p1), "+v"(e14), "+v"(e15), "+v"(sA), "+v"(o0), "+v"(o1))
#define DM(P) do { glds16<SC1>(rs, l16, goff + (P) * 4096, refill ? ((P) < 8 ? dsl + (P) * 4096 : dsl8) : ldsb + L_DUMMY); SB(); } while (0)
      int sl = 0;
      u32x4 fl;
      bool late = false;
#pragma unroll 1
      for (int c = 0; c < 3; ++c) {
        const bool chk = layer == 0 ? (c == 2) : (c == 0 && w == 0);
        const unsigned chks = chk ? 0u : 0x80000000u;
        const unsigned chko = layer == 0 ? (lane < 32 ? FLG_T + 2048 + w * 512 + l16 : FLG_PZ + R * 64 + (lane & 3) * 16) : FLG_KV + (8 + (blockIdx.x & 7)) * 1024 + 512 + (l16 & 0x1f0);
#define CHKLD() do { fl = ld16<SC1>(rs, chko, chks); SB(); } while (0)
        TILE(kX, vX0, vX1, kY, vY0, vY1, 1 * TB, , );
        TILE(kY, vY0, vY1, kX, vX0, vX1, 2 * TB, , );
        TILE(kX, vX0, vX1, kY, vY0, vY1, 3 * TB, , );
        TILE(kY, vY0, vY1, kX, vX0, vX1, 4 * TB, , );
        TILE(kX, vX0, vX1, kY, vY0, vY1, 5 * TB, , CHKLD());
        TILE(kY, vY0, vY1, kX, vX0, vX1, 6 * TB, , );
        TILE(kX, vX0, vX1, kY, vY0, vY1, 7 * TB, , );
        PINP();
        if (chk) {
          const bool ok = __all(pair_ok(fl, tag));
          if (layer == 0) { if (!ok) wait_pairs(rs, chko, 0, true, tag); }
          else if (tid == 0) *(volatile LAS unsigned*)(size_t)(ldsb + L_MISC) = ok ? 1u : 0u;
        }
        asm volatile("s_waitcnt vmcnt(0) lgkmcnt(0)\n\ts_barrier" ::: "memory");
        if (layer == 0 && c == 0 && tid == 0) put_tag((GAS u64*)(ws + FLG_PZ) + R * 8 + j, tag);
        if (layer == 0 && tid == 0 && !strm_b) {
          if (c == GATE_CH) put_tag((GAS u64*)(ws + FLG_G) + blockIdx.x, tag);
        }
        if (layer == 1 && c == 0) late = __builtin_amdgcn_readfirstlane(*(volatile LAS unsigned*)(size_t)(ldsb + L_MISC)) == 0u;
        if (late && c == 1) {
          __builtin_amdgcn_s_setprio(3);
          if (w == 0) wait_pairs(rs, l16 & 0x1f0, FLG_KV + (8 + (blockIdx.x & 7)) * 1024 + 512, true, tag);
          BARRIER();
#pragma unroll
          for (int p = 0; p < 9; ++p) DMA(p, kvb + 2 * SLOTB, 0);
          asm volatile("s_waitcnt vmcnt(0)\n\ts_barrier" ::: "memory");
          if (strm_b) __builtin_amdgcn_s_setprio(PB_ATT);
        }
        if (layer == 1 && c == 1 && !strm_b) __builtin_amdgcn_s_setprio(PA_ATT1B);
        SB();
        { const int d = (sl == 0 ? SLOTB : -SLOTB) / 16; kp += d; vp += d; }
        const bool refill = (c < 2) && !(late && c == 0);
        const unsigned goff = opq(refill ? kvb + (c + 2) * SLOTB + w * 1024 : 0x80000000u);
        const unsigned dsl = opq(ldsb + sl * SLOTB + w * 1024), dsl8 = opq(w < 2 ? ldsb + sl * SLOTB + w * 1024 + 8 * 4096 : ldsb + L_DUMMY);
        UNIT(sA, sB, kY, q1, o1, vX1, o0, vY0, DM(0); DM(1), LDN(kX, vX0, vX1, 0); DM(2), DM(3); DM(4));
        UNIT(sB, sA, kX, q0, o0, vY1, o1, vY0, DM(5); DM(6), DM(7), DM(8));
        sl ^= 1;
      }
      {
        const int k0 = w < 2 ? 22 * w : 44 + 21 * (w - 2);
        wob = opq(WO_T + ((2 * j) * 32 + 8 * w) * 1024);
        wf0 = opq(WGU_T + (fb0 + w) * 32 * 1024);
        wf1 = opq(WGU_T + (fb0 + w + 4) * 32 * 1024);
        wf2 = opq(w + 8 < nfb ? WGU_T + (fb0 + w + 8) * 32 * 1024 : 0x80000000u);
        db = opq(WD_T + ((2 * j) * 86 + k0) * 1024);
        zlast = opq(w < 2 ? 0u : 0x80000000u);
      }
#define PRE(U) do { if (2 * (U) < NPRE) { WLD(2 * (U)); WLD(2 * (U) + 1); SB(); } } while (0)
      TILE(kX, vX0, vX1, kY, vY0, vY1, 1 * TB, PRE(0), PRE(1));
      TILE(kY, vY0, vY1, kX, vX0, vX1, 2 * TB, PRE(2), PRE(3));
      TILE(kX, vX0, vX1, kY, vY0, vY1, 3 * TB, PRE(4), PRE(5));
      TILE(kY, vY0, vY1, kX, vX0, vX1, 4 * TB, PRE(6), PRE(7));
      TILE(kX, vX0, vX1, kY, vY0, vY1, 5 * TB, PRE(8), PRE(9));
      TILE(kY, vY0, vY1, kX, vX0, vX1, 6 * TB, PRE(10), PRE(11));
      TILE(kX, vX0, vX1, kY, vY0, vY1, 7 * TB, PRE(12), PRE(13));
      UNIT(sA, sB, kY, q1, o1, vX1, o0, vY0, , , );
      UNIT(sB, sA, kY, q0, o0, vY1, o1, vY0, , , );
      p1[3] = pk2(e14, e15);
      o1 = mfma32(vY1, p1, o1);
    }
    if (strm_b) __builtin_amdgcn_s_setprio(PB_CH); else __builtin_amdgcn_s_setprio(PA_CH);
    {
      STAGE_VARS();
      STAMP(2 + 12 * layer);
      const int tc = t < 16 ? t : (t == 16 ? 16 : 17);
      STAMP0N(42);
      f32x4* dst = (f32x4*)(lds + L_A + w * 3072);
      if (kh) { dst[lane] = (f32x4){o0[0], o0[1], o0[2], o0[3]}; dst[64 + lane] = (f32x4){o0[4], o0[5], o0[6], o0[7]}; dst[128 + lane] = (f32x4){o0[8], 0.f, 0.f, 0.f}; }
      else    { dst[lane] = (f32x4){o1[0], o1[1], o1[2], o1[3]}; dst[64 + lane] = (f32x4){o1[4], o1[5], o1[6], o1[7]}; dst[128 + lane] = (f32x4){o1[8], 0.f, 0.f, 0.f}; }
      BARRIER();
      const f32x4* src = (const f32x4*)(lds + L_A + (w ^ 2) * 3072);
      const f32x4 a = src[lane], b = src[64 + lane], cc = src[128 + lane];
      float k8[9];
#pragma unroll
      for (int i = 0; i < 9; ++i) k8[i] = kh ? o1[i] : o0[i];
      float r[8] = {k8[0] + a[0], k8[1] + a[1], k8[2] + a[2], k8[3] + a[3], k8[4] + b[0], k8[5] + b[1], k8[6] + b[2], k8[7] + b[3]};
      float l = sum32(k8[8] + cc[0]);
      const bool ok = (l >= 0x1p-60f) && (l <= 0x1p60f);
      if (!__all(ok)) {
        const u32x4 qf = kh ? q1 : q0;
        f32x16 o = {};
        float m = -INFINITY;
        for (int tile = 0; tile < NRB; ++tile) {
          const u32x4 kf = ld16<SC1>(rs, l16, kvb + tile * TB);
          const u32x4 v0 = ld16<SC1>(rs, hi * (VR / 2) + tc * 16, kvb + tile * TB + 1024);
          const u32x4 v1 = ld16<SC1>(rs, hi * (VR / 2) + tc * 16, kvb + tile * TB + 1024 + VR);
          const f32x16 z = {};
          const f32x16 s = mfma32(kf, qf, z);
          float mx = s[0];
#pragma unroll
          for (int i = 1; i < 16; ++i) mx = fmaxf(mx, s[i]);
          mx = fmaxf(max32(mx), m);
          const float f = EXP2(m - mx);
          m = mx;
#pragma unroll
          for (int i = 0; i < 16; ++i) o[i] *= f;
          u32x4 p0, p1;
#pragma unroll
          for (int e = 0; e < 4; ++e) {
            p0[e] = pk2(EXP2(s[2 * e] - m), EXP2(s[2 * e + 1] - m));
            p1[e] = pk2(EXP2(s[8 + 2 * e] - m), EXP2(s[8 + 2 * e + 1] - m));
          }
          o = mfma32(v0, p0, o);
          o = mfma32(v1, p1, o);
        }
        l = sum32(o[8]);
#pragma unroll
        for (int i = 0; i < 8; ++i) r[i] = o[i];
      }
      const float il = 1.f / l;
      const u32x4 ao = {pk2(r[0] * il, r[1] * il), pk2(r[2] * il, r[3] * il), pk2(r[4] * il, r[5] * il), pk2(r[6] * il, r[7] * il)};
      const int h = 4 * j + 2 * hp + kh;
      STAMP0N(43);
      SB();
      st16<SC1>(rs, unpoison(ao), l16, AOF + ((layer * 64 + R) * 32 + h) * 1024);
      SB();
#pragma unroll
      for (int n = NPRE; n < RING; ++n) WLD(n);
      SB();
      STAMP(3 + 12 * layer);
    }
    {
      STAGE_VARS();
      if (layer == 0) {
        const GAS float* u = (const GAS float*)(ws + CSGU) + fb0 * 32;
        for (int i = tid; i < nfb * 32; i += 256) {
          float s = 0.f;
#pragma unroll
          for (int p = 0; p < 8; ++p) s += __hip_atomic_load(u + p * 86 * 32 + i, __ATOMIC_RELAXED, __HIP_MEMORY_SCOPE_AGENT);
          ((float*)(lds + L_CSGU))[i] = s;
        }
      }
      const unsigned ab = opq(AOF + ((layer * 64 + R) * 32 + 8 * w) * 1024);
      u32x4 b[8];
      poll_frags<8>(rs, l16, ab, b);
      STAMP(4 + 12 * layer);
      f32x16 acc[2] = {};
#pragma unroll
      for (int i = 0; i < 8; ++i) { acc[0] = mfma32(W[2 * i], b[i], acc[0]); acc[1] = mfma32(W[2 * i + 1], b[i], acc[1]); }
      asm volatile("" : "+v"(acc[0]), "+v"(acc[1]));
      STAMP0N(32);
      red_write<2>(lds + L_B, w, lane, acc);
      BARRIER();
      STAMP0N(33);
      float v[8]; red_read8<2>(lds + L_B, w >> 1, w & 1, lane, v);
#pragma unroll
      for (int e = 0; e < 4; ++e) xr[e] = pk2(v[2 * e] + bflo(xr[e]), v[2 * e + 1] + bfhi(xr[e]));
      xr = unpoison(xr);
      asm volatile("" : "+v"(xr));
      STAMP0N(34);
      SB();
      st16<SC1>(rs, xr, l16, X1F + ((layer * 64 + R) * 32 + 4 * j + w) * 1024);
      SB();
#pragma unroll
      for (int n = 0; n < 16; ++n) WLD(RING + n);
      SB();
      STAMP(5 + 12 * layer);
    }
    {
      STAGE_VARS();
      const unsigned xb = opq(X1F + ((layer * 64 + R) * 32 + 8 * w) * 1024);
      {
        u32x4 b[8];
        poll_frags<8>(rs, l16, xb, b);
        STAMP(6 + 12 * layer);
        float s1 = 0.f, s2 = 0.f;
#pragma unroll
        for (int i = 0; i < 8; ++i) {
          *(u32x4*)(lds + L_A + (8 * w + i) * 1024 + l16) = b[i];
#pragma unroll
          for (int e = 0; e < 4; ++e) { s1 = dot2(b[i][e], ONES2, s1); s2 = dot2(b[i][e], b[i][e], s2); }
        }
        s1 = sum32(s1); s2 = sum32(s2);
        if (hi == 0) ((f32x2*)(lds + L_ST))[w * 32 + t] = (f32x2){s1, s2};
      }
      STAMP0N(35);
      BARRIER();
      STAMP0N(36);
      float mean, rstd; ln_stats(st, t, mean, rstd);
      const float mr = mean * rstd;
      const unsigned hb0 = opq(HF + ((layer * 64 + R) * 86 + fb0 + w) * 1024);
      const float* csb = (const float*)(lds + L_CSGU) + w * 32 + 4 * hi;
      f32x16 accA, accB;
      float h[8];
      f32x4 cg0, cg1, cu0, cu1;
#define CSLD(BLK) do { cg0 = *(const f32x4*)(csb + (BLK) * 128); cg1 = *(const f32x4*)(csb + (BLK) * 128 + 8); \
        cu0 = *(const f32x4*)(csb + (BLK) * 128 + 16); cu1 = *(const f32x4*)(csb + (BLK) * 128 + 24); } while (0)
#define EPI(ACC, BLK, E) do { const float cg_ = (E) < 4 ? cg0[(E) & 3] : cg1[(E) & 3], cu_ = (E) < 4 ? cu0[(E) & 3] : cu1[(E) & 3]; \
        h[E] = geglu(ACC[E] * rstd - mr * cg_, ACC[8 + (E)] * rstd - mr * cu_); asm volatile("" : "+v"(h[E])); } while (0)
#define HST(BLK) do { if (w + 4 * (BLK) < nfb) { const u32x4 hv_ = pack8(h); st16<SC1>(rs, unpoison(hv_), l16, hb0 + (BLK) * 4096); } } while (0)
#define XB(KS) (*(const u32x4*)(lds + L_A + ((KS) & 31) * 1024 + l16))
#define FBLOCK(ACC, BLK, HASP, PACC, PBLK) do {                                                        \
        _Pragma("unroll") for (int ks = 0; ks < 32; ++ks) {                                             \
          const int s_ = 16 + 32 * (BLK) + ks;                                                          \
          const u32x4 bb_ = bq[ks & 3];                                                                 \
          ACC = mfma32(W[s_ % RING], bb_, ks == 0 ? zf : ACC);                                          \
          SB();                                                                                         \
          if ((BLK) < 2 || ks + 4 < 32) bq[ks & 3] = XB(ks + 4);                                        \
          WLD(s_ + RING);                                                                               \
          SB();                                                                                         \
          if (HASP && (ks & 3) == 1) EPI(PACC, PBLK, ks >> 2);                                          \
          SB();                                                                                         \
        }                                                                                               \
        if (HASP) HST(PBLK);                                                                            \
      } while (0)
      u32x4 bq[4];
#pragma unroll
      for (int i = 0; i < 4; ++i) bq[i] = XB(i);
      const f32x16 zf = {};
      FBLOCK(accA, 0, false, accA, 0);
      STAMP0N(44);
      CSLD(0);
      FBLOCK(accB, 1, true, accA, 0);
      STAMP0N(45);
      CSLD(1);
      FBLOCK(accA, 2, true, accB, 1);
      CSLD(2);
      STAMP0N(37);
#pragma unroll
      for (int e = 0; e < 8; ++e) EPI(accA, 2, e);
      HST(2);
      STAMP0N(39);
      STAMP(7 + 12 * layer);
    }
    {
      STAGE_VARS();
      const int k0 = w < 2 ? 22 * w : 44 + 21 * (w - 2), nk = w < 2 ? 22 : 21;
      const unsigned hb = opq(HF + ((layer * 64 + R) * 86 + k0) * 1024);
      prepoll(rs, hb, nk, lane);
      STAMP(8 + 12 * layer);
      f32x16 acc[2] = {};
      u32x4 b0[6], b1[8], b2[8];
#define LB(B, N, I0) do { _Pragma("unroll") for (int i = 0; i < (N); ++i) B[i] = ld16<SC1>(rs, l16, ((I0) + i < 21 ? 0u : zlast) | (hb + ((I0) + i) * 1024)); } while (0)
#define MM(B, N, I0) do { _Pragma("unroll") for (int i = 0; i < (N); ++i) { const int s_ = 112 + 2 * ((I0) + i); \
          acc[0] = mfma32(W[s_ % RING], B[i], acc[0]); acc[1] = mfma32(W[(s_ + 1) % RING], B[i], acc[1]); WLD(s_ + RING); WLD(s_ + 1 + RING); SB(); } } while (0)
#define CK(B, N, I0) do { for (unsigned spins = 0; spins < SPIN_CAP && !frags_ok<N>(B); ++spins) { asm volatile("" ::: "memory"); LB(B, N, I0); } } while (0)
      LB(b0, 6, 0); LB(b1, 8, 6); SB();
      CK(b0, 6, 0);
      STAMP0N(46);
      MM(b0, 6, 0);
      LB(b2, 8, 14); SB();
      CK(b1, 8, 6);
      MM(b1, 8, 6);
      STAMP0N(47);
      CK(b2, 8, 14);
      MM(b2, 8, 14);
      asm volatile("" : "+v"(acc[0]), "+v"(acc[1]));
      STAMP0N(40);
      red_write<2>(lds + L_B, w, lane, acc);
      BARRIER();
      STAMP0N(41);
      float v[8]; red_read8<2>(lds + L_B, w >> 1, w & 1, lane, v);
#pragma unroll
      for (int e = 0; e < 4; ++e) xr[e] = pk2(v[2 * e] + bflo(xr[e]), v[2 * e + 1] + bfhi(xr[e]));
      if (layer == 1) {
        uint16_t* p = out + (size_t)(32 * R + t) * DM + 16 * (4 * j + w) + 4 * hi;
        *(u32x2*)p = (u32x2){xr[0], xr[1]};
        *(u32x2*)(p + 8) = (u32x2){xr[2], xr[3]};
        if (blockIdx.x == 0 && tid == 0) put_tag((GAS u64*)(ws + NONCE), tag);
        STAMP(9 + 12 * layer);
        CSTAMP(31);
      }
    }
    if (layer == 0) {
      STAGE_VARS();
      u32x4 wqr[16], wkr[8];
      xr = unpoison(xr);
      st16<SC1>(rs, xr, l16, X2F + (R * 32 + 4 * j + w) * 1024);
      SB();
      const unsigned qb = opq(WQ_T + ((2 * j) * 32 + 8 * w) * 1024), kb = opq((role ? 0u : 0x80000000u) | (WKV_T + 8 * w * 1024));
#pragma unroll
      for (int i = 0; i < 8; ++i) wkr[i] = ld16<SC1>(rs, l16, kb + i * 1024);
#pragma unroll
      for (int i = 0; i < 8; ++i) {
        wqr[2 * i] = ld16<SC1>(rs, l16, qb + i * 1024);
        wqr[2 * i + 1] = ld16<SC1>(rs, l16, qb + (32 + i) * 1024);
      }
      SB();
      STAMP(9 + 12 * layer);
      const unsigned x2b = opq(X2F + (R * 32 + 8 * w) * 1024);
      u32x4 b[8];
      poll_frags<8>(rs, l16, x2b, b);
      STAMP(10 + 12 * layer);
      f32x16 acc[3] = {};
      float s1 = 0.f, s2 = 0.f;
      if (role == 1) {
#pragma unroll
        for (int i = 0; i < 8; ++i) acc[2] = mfma32(wkr[i], b[i], acc[2]);
      } else if (role == 2) {
#pragma unroll
        for (int i = 0; i < 8; ++i) acc[2] = mfma32(b[i], wkr[i], acc[2]);
      }
#pragma unroll
      for (int i = 0; i < 8; ++i) {
        acc[0] = mfma32(wqr[2 * i], b[i], acc[0]); acc[1] = mfma32(wqr[2 * i + 1], b[i], acc[1]);
#pragma unroll
        for (int e = 0; e < 4; ++e) { s1 = dot2(b[i][e], ONES2, s1); s2 = dot2(b[i][e], b[i][e], s2); }
      }
      s1 = sum32(s1); s2 = sum32(s2);
      BARRIER();
      if (hi == 0) ((f32x2*)(lds + L_ST))[w * 32 + t] = (f32x2){s1, s2};
      red_write<3>(lds, w, lane, acc);
      BARRIER();
      float mean, rstd; ln_stats(st, t, mean, rstd);
      if (role && w == role - 1) {
        const float* ck = (const float*)(lds + L_CSKV);
        auto cs = [&](int i) { return ck[i]; };
        auto rd = [&](int s, float* v) { red_read8<3>(lds, 2, s, role == 1 ? lane : (lane | 16), v); };
        kv_finish(rs, role, rd, mean, rstd, cs, KV0 + KVL + kvpos(R) * TB, lane, lds + L_MISC + 16);
        WAITV0();
        kv_publish(ws, 1, kvpos(R), role, tag, lane);
      }
      {
        float v[8];
        const float* c = (const float*)(lds + L_CSQ) + 32 * hp + 4 * hi;
        red_read8<3>(lds, hp, 0, lane, v);
        q0 = ln_pack(v, *(const f32x4*)c, *(const f32x4*)(c + 8), mean, rstd * QSCALE);
        red_read8<3>(lds, hp, 1, lane, v);
        q1 = ln_pack(v, *(const f32x4*)(c + 16), *(const f32x4*)(c + 24), mean, rstd * QSCALE);
      }
      BARRIER();
      STAMP(11 + 12 * layer);
      if (w == 0) wait_pairs(rs, l16 & 0x1f0, FLG_KV + (8 + (blockIdx.x & 7)) * 1024, true, tag);
      BARRIER();
      STAMP(12 + 12 * layer);
#pragma unroll
      for (int p = 0; p < 9; ++p) DMA(p, KV0 + KVL, 0);
    }
  }
  STAMPDUMP();
}

extern "C" void kernel_launch(void* const* d_in, const int* in_sizes, int n_in, void* d_out, int out_size, void* d_ws,
                              size_t ws_size, hipStream_t stream) {
  const uint16_t* x = (const uint16_t*)d_in[0];
  const uint16_t* wq = (const uint16_t*)d_in[1];
  const uint16_t* wk = (const uint16_t*)d_in[2];
  const uint16_t* wv = (const uint16_t*)d_in[3];
  const uint16_t* wo = (const uint16_t*)d_in[4];
  const uint16_t* wg = (const uint16_t*)d_in[6];
  const uint16_t* wu = (const uint16_t*)d_in[7];
  const uint16_t* wd = (const uint16_t*)d_in[8];
  (void)hipFuncSetAttribute((const void*)k_main, hipFuncAttributeMaxDynamicSharedMemorySize, L_END);
  k_main<<<512, 256, L_END, stream>>>(x, wq, wk, wv, wo, wg, wu, wd, (char*)d_ws, (uint16_t*)d_out);
}
```

```cpp
#include <hip/hip_runtime.h>
#include <hip/hip_bf16.h>
#include <stdint.h>

typedef float f32x2 __attribute__((ext_vector_type(2)));
typedef float f32x4 __attribute__((ext_vector_type(4)));
typedef float f32x16 __attribute__((ext_vector_type(16)));
typedef __bf16 bf16x2 __attribute__((ext_vector_type(2)));
typedef __bf16 bf16x8 __attribute__((ext_vector_type(8)));
typedef short s16x4 __attribute__((ext_vector_type(4)));
typedef unsigned u32x2 __attribute__((ext_vector_type(2)));
typedef unsigned u32x4 __attribute__((ext_vector_type(4)));
typedef unsigned long long u64;
typedef __amdgpu_buffer_rsrc_t rsrc_t;
#define DI __device__ __forceinline__
#define LAS __attribute__((address_space(3)))
#define GAS __attribute__((address_space(1)))
#define SB() __builtin_amdgcn_sched_barrier(0)
#define EXP2(x) __builtin_amdgcn_exp2f(x)

constexpr int SEQ = 2048, DM = 512, FF = 1376, NRB = 64, NFB = 86;
constexpr int VR = 576;
constexpr int TB = 1024 + 2 * VR;
constexpr int SLOTB = 16 * TB;
constexpr unsigned WQ_T = 0;
constexpr unsigned WKV_T = WQ_T + 16 * 32 * 1024;
constexpr unsigned WO_T = WKV_T + 32 * 1024;
constexpr unsigned WGU_T = WO_T + 16 * 32 * 1024;
constexpr unsigned WD_T = WGU_T + 86 * 32 * 1024;
constexpr unsigned CSGU = WD_T + 16 * 86 * 1024;
constexpr unsigned KV0 = (CSGU + 8 * 86 * 32 * 4 + 4095) / 4096 * 4096;
constexpr unsigned KVL = NRB * TB;
constexpr unsigned AOF = KV0 + 2 * KVL;
constexpr unsigned X1F = AOF + 2 * NRB * 32 * 1024;
constexpr unsigned HF = X1F + 2 * NRB * 32 * 1024;
constexpr unsigned X2F = HF + 2 * NRB * 86 * 1024;
constexpr unsigned FLG_PZ = X2F + NRB * 32 * 1024;
constexpr unsigned FLG_KV = FLG_PZ + 2 * 32 * 64;
constexpr unsigned FLG_T = FLG_KV + 2 * 8 * NRB * 16;
constexpr unsigned NONCE = FLG_T + 256 * 8;
constexpr unsigned WS_END = NONCE + 128;

constexpr unsigned L_RED = 0;
constexpr unsigned L_WO = 2 * SLOTB;
constexpr unsigned L_WKV = 135168;
constexpr unsigned L_XR = L_WKV;
constexpr unsigned L_TAB = 151552;
constexpr unsigned L_CSQ = L_TAB;
constexpr unsigned L_CSKV = L_CSQ + 256;
constexpr unsigned L_CSGU = L_CSKV + 128;
constexpr unsigned L_ST = L_CSGU + 11 * 128;
constexpr unsigned L_SCR = L_ST + 4096;
constexpr unsigned L_CSP = L_SCR + 512;
constexpr unsigned L_DUMMY = L_SCR + 512;
constexpr unsigned L_END = 163840;
static_assert(L_DUMMY % 16 == 0 && L_CSP + 1536 + 256 <= L_END, "LDS map");

DI f32x16 mfma32(u32x4 a, u32x4 b, f32x16 c) {
  return __builtin_amdgcn_mfma_f32_32x32x16_bf16(__builtin_bit_cast(bf16x8, a), __builtin_bit_cast(bf16x8, b), c, 0, 0, 0);
}
DI unsigned pk2(float a, float b) {
  f32x2 v = {a, b};
  return __builtin_bit_cast(unsigned, __builtin_convertvector(v, bf16x2));
}
DI float bflo(unsigned u) { return __uint_as_float(u << 16); }
DI float bfhi(unsigned u) { return __uint_as_float(u & 0xffff0000u); }
DI float sum32(float v) {
  auto r = __builtin_amdgcn_permlane32_swap(__float_as_uint(v), __float_as_uint(v), false, false);
  return __uint_as_float(r[0]) + __uint_as_float(r[1]);
}
DI float max32(float v) {
  auto r = __builtin_amdgcn_permlane32_swap(__float_as_uint(v), __float_as_uint(v), false, false);
  return fmaxf(__uint_as_float(r[0]), __uint_as_float(r[1]));
}
DI rsrc_t make_rsrc(const void* p, unsigned bytes) { return __builtin_amdgcn_make_buffer_rsrc((void*)p, 0, bytes, 0x00020000); }
constexpr int SC1 = 16;
template <int AUX> DI u32x4 ld16(rsrc_t r, unsigned voff, unsigned soff) { return __builtin_amdgcn_raw_buffer_load_b128(r, voff, soff, AUX); }
template <int AUX> DI void st16(rsrc_t r, u32x4 v, unsigned voff, unsigned soff) {
  __builtin_amdgcn_raw_buffer_store_b128(v, r, voff, soff, AUX);
  asm volatile("s_nop 1" :: "v"(v));
}
DI u32x2 ld8(rsrc_t r, unsigned voff, unsigned soff) { return __builtin_amdgcn_raw_buffer_load_b64(r, voff, soff, 0); }
template <int AUX> DI void glds16(rsrc_t r, unsigned voff, unsigned soff, unsigned ldsaddr) {
  __builtin_amdgcn_raw_ptr_buffer_load_lds(r, (LAS void*)(size_t)ldsaddr, 16, voff, soff, 0, AUX);
}
DI float dot2(unsigned a, unsigned b, float c) {
  return __builtin_amdgcn_fdot2_f32_bf16(__builtin_bit_cast(bf16x2, a), __builtin_bit_cast(bf16x2, b), c, false);
}
constexpr unsigned ONES2 = 0x3f803f80u;
DI unsigned opq(unsigned x) { x = __builtin_amdgcn_readfirstlane(x); asm volatile("" : "+s"(x)); return x; }
DI u32x2 tr8(unsigned ldsaddr) {
  return __builtin_bit_cast(u32x2, __builtin_amdgcn_ds_read_tr16_b64_v4i16((LAS s16x4*)(size_t)ldsaddr));
}
DI u32x4 tr_frag(unsigned a, unsigned row_stride) {
  const u32x2 lo = tr8(a), hi = tr8(a + 8 * row_stride);
  return (u32x4){lo[0], lo[1], hi[0], hi[1]};
}
DI u32x4 tr_frag_nat(unsigned a, unsigned row_stride) {
  const u32x2 lo = tr8(a), hi = tr8(a + 4 * row_stride);
  return (u32x4){lo[0], lo[1], hi[0], hi[1]};
}
DI float frag_sum(u32x4 f, float s) {
#pragma unroll
  for (int e = 0; e < 4; ++e) s = dot2(f[e], ONES2, s);
  return s;
}
#define WAITV0() asm volatile("s_waitcnt vmcnt(0)" ::: "memory")
#define BARRIER() asm volatile("s_waitcnt lgkmcnt(0)\n\ts_barrier" ::: "memory")

#define XSPLIT 1
DI void decode_block(int b, int& G, int& j) {
  const int x = b & 7, r = b >> 3;
  constexpr int MPX = 8 / XSPLIT;
  j = MPX * (x % XSPLIT) + r % MPX;
  G = 4 * XSPLIT * (x / XSPLIT) + r / MPX;
}

DI void red_write(char* lds, int w, int lane, const f32x16* acc) {
#pragma unroll
  for (int t = 0; t < 4; ++t)
#pragma unroll
    for (int q = 0; q < 4; ++q)
      *(f32x4*)(lds + w * 16384 + ((t * 4 + q) * 64 + lane) * 16) = (f32x4){acc[t][4 * q], acc[t][4 * q + 1], acc[t][4 * q + 2], acc[t][4 * q + 3]};
}
template <int WS, int QS> DI void sum8(const char* p, float* v) {
#pragma unroll
  for (int qq = 0; qq < 2; ++qq) {
    f32x4 a = *(const f32x4*)(p + qq * QS), b = *(const f32x4*)(p + qq * QS + WS);
#pragma unroll
    for (int w = 2; w < 8; w += 2) { a += *(const f32x4*)(p + qq * QS + w * WS); b += *(const f32x4*)(p + qq * QS + (w + 1) * WS); }
    a += b;
    v[4 * qq] = a[0]; v[4 * qq + 1] = a[1]; v[4 * qq + 2] = a[2]; v[4 * qq + 3] = a[3];
  }
}
template <int NT> DI void red4_write(char* lds, int w, int lane, const f32x16* acc) {
#pragma unroll
  for (int t = 0; t < NT; ++t)
#pragma unroll
    for (int q = 0; q < 4; ++q)
      *(f32x4*)(lds + w * (NT * 4096) + ((t * 4 + q) * 64 + lane) * 16) = (f32x4){acc[t][4 * q], acc[t][4 * q + 1], acc[t][4 * q + 2], acc[t][4 * q + 3]};
}
template <int WS, int QS> DI void sum4(const char* p, float* v) {
#pragma unroll
  for (int qq = 0; qq < 2; ++qq) {
    const f32x4 a = (*(const f32x4*)(p + qq * QS) + *(const f32x4*)(p + qq * QS + WS)) + (*(const f32x4*)(p + qq * QS + 2 * WS) + *(const f32x4*)(p + qq * QS + 3 * WS));
    v[4 * qq] = a[0]; v[4 * qq + 1] = a[1]; v[4 * qq + 2] = a[2]; v[4 * qq + 3] = a[3];
  }
}
template <int NT> DI void red4_read8(const char* lds, int tt, int t, int s, int lane, float* v) {
  sum4<NT * 4096, 1024>(lds + 4 * tt * (NT * 4096) + ((t * 4 + 2 * s) * 64 + lane) * 16, v);
}
DI void red_read8(const char* lds, int t, int s, int lane, float* v) {
  sum8<16384, 1024>(lds + ((t * 4 + 2 * s) * 64 + lane) * 16, v);
}
constexpr unsigned POISON = 0xffffffffu;
DI u32x4 poison4() { return (u32x4){POISON, POISON, POISON, POISON}; }
DI u32x4 pack8(const float* h) { return (u32x4){pk2(h[0], h[1]), pk2(h[2], h[3]), pk2(h[4], h[5]), pk2(h[6], h[7])}; }
DI u32x4 unpoison(u32x4 v) { return (u32x4){min(v[0], POISON - 1), min(v[1], POISON - 1), min(v[2], POISON - 1), min(v[3], POISON - 1)}; }
template <int N> DI bool frags_ok(const u32x4* b) {
  unsigned m = 0;
#pragma unroll
  for (int i = 0; i < N; ++i) m = max(max(max(m, b[i][0]), max(b[i][1], b[i][2])), b[i][3]);
  return __all(m != POISON);
}
DI void ln_stats(const f32x2* st, int tt, int tok, float& mean, float& rstd) {
  f32x2 a = st[tt * 32 + tok];
#pragma unroll
  for (int w = 1; w < 8; ++w) a += st[(w * 2 + tt) * 32 + tok];
  mean = a[0] * (1.f / 512.f);
  rstd = __builtin_amdgcn_rsqf(fmaxf(a[1] * (1.f / 512.f) - mean * mean, 0.f) + 1e-5f);
}
DI u32x4 ln_pack(const float* v, f32x4 c0, f32x4 c1, float mean, float sc) {
  return (u32x4){pk2((v[0] - mean * c0[0]) * sc, (v[1] - mean * c0[1]) * sc), pk2((v[2] - mean * c0[2]) * sc, (v[3] - mean * c0[3]) * sc),
                 pk2((v[4] - mean * c1[0]) * sc, (v[5] - mean * c1[1]) * sc), pk2((v[6] - mean * c1[2]) * sc, (v[7] - mean * c1[3]) * sc)};
}
constexpr float QSCALE = 0.25f * 1.4426950408889634f;

DI void put_tag(GAS u64* p, u64 tag) {
  asm volatile("" : "+s"(tag));
  __hip_atomic_store(p, tag, __ATOMIC_RELAXED, __HIP_MEMORY_SCOPE_AGENT);
}
constexpr unsigned SPIN_CAP = 1u << 17;
DI bool pair_ok(u32x4 v, u64 tag) { return v[0] == (unsigned)tag && v[1] == (unsigned)(tag >> 32) && v[2] == (unsigned)tag && v[3] == (unsigned)(tag >> 32); }
DI void wait_pairs(rsrc_t rs, unsigned voff, unsigned soff, bool active, u64 tag) {
  for (unsigned spins = 0; spins < SPIN_CAP; ++spins) {
    asm volatile("" ::: "memory");
    const u32x4 v = ld16<SC1>(rs, voff, soff);
    if (__all(!active || pair_ok(v, tag))) return;
    __builtin_amdgcn_s_sleep(4);
  }
}
DI void kv_publish(char* ws, int layer, int pos, int role, u64 tag, int lane) {
  if (lane < 8) put_tag((GAS u64*)(ws + FLG_KV) + ((layer * 8 + lane) * 64 + pos) * 2 + (role - 1), tag);
}
DI void geglu4(float& g0, float& g1, float& g2, float& g3, float u0, float u1, float u2, float u3, f32x4 cg, f32x4 cu, float r, float m, float c1, float c2) {
  float q0, q1, q2, q3;
  asm volatile(
      "s_nop 7\n\t"
      "v_mul_f32 %8, %21, %12\n\tv_mul_f32 %9, %21, %13\n\tv_mul_f32 %10, %21, %14\n\tv_mul_f32 %11, %21, %15\n\t"
      "v_fma_f32 %0, %0, %20, -%8\n\tv_fma_f32 %1, %1, %20, -%9\n\tv_fma_f32 %2, %2, %20, -%10\n\tv_fma_f32 %3, %3, %20, -%11\n\t"
      "v_mul_f32 %8, %21, %16\n\tv_mul_f32 %9, %21, %17\n\tv_mul_f32 %10, %21, %18\n\tv_mul_f32 %11, %21, %19\n\t"
      "v_fma_f32 %4, %4, %20, -%8\n\tv_fma_f32 %5, %5, %20, -%9\n\tv_fma_f32 %6, %6, %20, -%10\n\tv_fma_f32 %7, %7, %20, -%11\n\t"
      "v_mul_f32 %8, %0, %0\n\tv_mul_f32 %9, %1, %1\n\tv_mul_f32 %10, %2, %2\n\tv_mul_f32 %11, %3, %3\n\t"
      "v_fma_f32 %8, %8, %23, %22\n\tv_fma_f32 %9, %9, %23, %22\n\tv_fma_f32 %10, %10, %23, %22\n\tv_fma_f32 %11, %11, %23, %22\n\t"
      "v_mul_f32 %8, %0, %8\n\tv_mul_f32 %9, %1, %9\n\tv_mul_f32 %10, %2, %10\n\tv_mul_f32 %11, %3, %11\n\t"
      "v_exp_f32 %8, %8\n\tv_exp_f32 %9, %9\n\tv_exp_f32 %10, %10\n\tv_exp_f32 %11, %11\n\t"
      "v_mul_f32 %4, %0, %4\n\tv_mul_f32 %5, %1, %5\n\tv_mul_f32 %6, %2, %6\n\tv_mul_f32 %7, %3, %7\n\t"
      "v_add_f32 %8, 1.0, %8\n\tv_add_f32 %9, 1.0, %9\n\tv_add_f32 %10, 1.0, %10\n\tv_add_f32 %11, 1.0, %11\n\t"
      "v_rcp_f32 %8, %8\n\tv_rcp_f32 %9, %9\n\tv_rcp_f32 %10, %10\n\tv_rcp_f32 %11, %11\n\t"
      "v_mul_f32 %0, %4, %8\n\tv_mul_f32 %1, %5, %9\n\tv_mul_f32 %2, %6, %10\n\tv_mul_f32 %3, %7, %11"
      : "+v"(g0), "+v"(g1), "+v"(g2), "+v"(g3), "+v"(u0), "+v"(u1), "+v"(u2), "+v"(u3), "=&v"(q0), "=&v"(q1), "=&v"(q2), "=&v"(q3)
      : "v"(cg[0]), "v"(cg[1]), "v"(cg[2]), "v"(cg[3]), "v"(cu[0]), "v"(cu[1]), "v"(cu[2]), "v"(cu[3]), "v"(r), "v"(m), "v"(c1), "s"(c2));
}
DI u32x4 geglu8(const f32x16& a, f32x4 cg0, f32x4 cg1, f32x4 cu0, f32x4 cu1, float r, float m) {
  const float c1 = -2.f * 0.7978845608028654f * 1.4426950408889634f, c2 = c1 * 0.044715f;
  float h[8] = {a[0], a[1], a[2], a[3], a[4], a[5], a[6], a[7]};
  geglu4(h[0], h[1], h[2], h[3], a[8], a[9], a[10], a[11], cg0, cu0, r, m, c1, c2);
  geglu4(h[4], h[5], h[6], h[7], a[12], a[13], a[14], a[15], cg1, cu1, r, m, c1, c2);
  return pack8(h);
}
template <int TS> DI void prepoll(rsrc_t rs, unsigned soff, int n, int lane) {
  const int f = min(lane, 2 * n - 1);
  const unsigned po = (f >= n ? TS + (f - n) * 1024 : f * 1024) + 1020;
  for (unsigned spins = 0; spins < SPIN_CAP; ++spins) {
    asm volatile("" ::: "memory");
    const unsigned v = __builtin_amdgcn_raw_buffer_load_b32(rs, po, soff, SC1);
    if (__all(v != POISON)) return;
    __builtin_amdgcn_s_sleep(1);
  }
}
template <int N> DI void poll_frags1(rsrc_t rs, unsigned voff, unsigned soff, u32x4* b) {
  for (unsigned spins = 0; spins < SPIN_CAP; ++spins) {
    asm volatile("" ::: "memory");
#pragma unroll
    for (int i = 0; i < N; ++i) b[i] = ld16<SC1>(rs, voff, soff + i * 1024);
    if (frags_ok<N>(b)) return;
  }
}
template <int N, int TS> DI void poll_frags(rsrc_t rs, unsigned voff, unsigned soff, u32x4* b) {
  for (unsigned spins = 0; spins < SPIN_CAP; ++spins) {
    asm volatile("" ::: "memory");
#pragma unroll
    for (int i = 0; i < 2 * N; ++i) b[i] = ld16<SC1>(rs, voff, soff + (i / N) * TS + (i % N) * 1024);
    if (frags_ok<2 * N>(b)) return;
  }
}

template <class RD, class CS>
DI void kv_finish(rsrc_t rs, int role, RD rd, float mean, float rstd, CS cs, unsigned kvbase, int lane, char* scr) {
  const int hi = lane >> 5, t = lane & 31;
  if (role == 1) {
    float v[8]; rd(0, v);
    const f32x4 c0 = {cs(4 * hi), cs(4 * hi + 1), cs(4 * hi + 2), cs(4 * hi + 3)}, c1 = {cs(8 + 4 * hi), cs(9 + 4 * hi), cs(10 + 4 * hi), cs(11 + 4 * hi)};
    st16<SC1>(rs, ln_pack(v, c0, c1, mean, rstd), lane * 16, kvbase);
  } else {
    float* sm = (float*)scr;
    if (hi == 0) { sm[t] = rstd; sm[32 + t] = mean * rstd; }
    asm volatile("s_waitcnt lgkmcnt(0)" ::: "memory");
    const float c = cs(t & 15);
#pragma unroll
    for (int s = 0; s < 2; ++s) {
      float v[8]; rd(s, v);
      const f32x4 r0 = *(const f32x4*)(sm + 16 * s + 4 * hi), r1 = *(const f32x4*)(sm + 16 * s + 8 + 4 * hi);
      const f32x4 m0 = *(const f32x4*)(sm + 32 + 16 * s + 4 * hi), m1 = *(const f32x4*)(sm + 32 + 16 * s + 8 + 4 * hi);
      u32x4 o = {pk2(v[0] * r0[0] - m0[0] * c, v[1] * r0[1] - m0[1] * c), pk2(v[2] * r0[2] - m0[2] * c, v[3] * r0[3] - m0[3] * c),
                 pk2(v[4] * r1[0] - m1[0] * c, v[5] * r1[1] - m1[1] * c), pk2(v[6] * r1[2] - m1[2] * c, v[7] * r1[3] - m1[3] * c)};
      if (t >= 16) { const unsigned k = t == 16 ? ONES2 : 0u; o = (u32x4){k, k, k, k}; }
      if (t < 18) st16<SC1>(rs, o, hi * (VR / 2) + t * 16, kvbase + 1024 + s * VR);
    }
  }
}
#define STAGE_VARS()                                                                                              \
  int tid = threadIdx.x; asm volatile("" : "+v"(tid));                                                            \
  int G = G_, j = j_, w = w_; asm volatile("" : "+s"(G), "+s"(j), "+s"(w));                                       \
  const int lane = tid & 63, hi = lane >> 5, t = lane & 31;                                                       \
  const int att = w & 1, hp = (w >> 1) & 1, kh = w >> 2;           \
  const int ftt = w >> 2, fx = w & 3;                          \
  const unsigned l16 = lane * 16;                                                                                 \
  const int nfb = j < 6 ? 11 : 10, fb0 = j < 6 ? 11 * j : 66 + 10 * (j - 6);                                      \
  const int role = j == (G & 7) ? 1 : (j == ((G + 1) & 7) ? 2 : 0);          \
  (void)hi; (void)t; (void)att; (void)hp; (void)kh; (void)ftt; (void)fx; (void)l16; (void)nfb; (void)fb0; (void)G; (void)role;

__global__ __launch_bounds__(512, 2) void k_main(const uint16_t* __restrict__ x, const uint16_t* __restrict__ wq,
                                                 const uint16_t* __restrict__ wk, const uint16_t* __restrict__ wv,
                                                 const uint16_t* __restrict__ wo, const uint16_t* __restrict__ wg,
                                                 const uint16_t* __restrict__ wu, const uint16_t* __restrict__ wd,
                                                 char* __restrict__ ws, uint16_t* __restrict__ out) {
  extern __shared__ __attribute__((aligned(16))) char lds[];
  int G_, j_; decode_block(blockIdx.x, G_, j_);
  const int w_ = __builtin_amdgcn_readfirstlane(threadIdx.x >> 6);
  const rsrc_t rs = make_rsrc(ws, WS_END);
  const unsigned ldsb = (unsigned)(size_t)(LAS char*)lds;
  const f32x2* st = (const f32x2*)(lds + L_ST);
  u32x4 q0, q1;
  u64 tag;
#define DMA(I, GOFF, SLOT) do { if ((I) < 4 || w < 2) glds16<SC1>(rs, l16, (GOFF) + w * 1024 + (I) * 8192, ldsb + (SLOT) * SLOTB + w * 1024 + (I) * 8192); } while (0)
  {
    STAGE_VARS();
    const u32x2 nv = *(const GAS u32x2*)(ws + NONCE);
    if (role) {
      const rsrc_t rk = make_rsrc(role == 1 ? wk : wv, DM * 16 * 2);
#pragma unroll
      for (int n = 0; n < 2; ++n) glds16<0>(rk, l16, (128 * fx + 64 * ftt + 32 * n) * 32, ldsb + L_WKV + fx * 4096 + ftt * 2048 + n * 1024);
    }
    {
      const rsrc_t rx = make_rsrc(x, SEQ * DM * 2);
#pragma unroll
      for (int n = 0; n < 8; ++n) glds16<0>(rx, (lane >> 4) * 1024 + (lane & 15) * 16, (2 * G + ftt) * 32768 + n * 4096 + fx * 256, ldsb + w * 8320 + n * 1040);
    }
    WAITV0();
    {
      const rsrc_t rq = make_rsrc(wq, DM * DM * 2);
#pragma unroll
      for (int n = 0; n < 8; ++n)
        glds16<0>(rq, (lane >> 3) * 1024 + ((lane & 7) ^ (((lane >> 4) & 1) << 2)) * 16, (128 * fx + 64 * ftt + 8 * n) * 1024 + j * 128,
                  ldsb + L_WO + fx * 16384 + (64 * ftt + 8 * n) * 128);
    }
    SB();
    u32x4 xb[8];
#pragma unroll
    for (int i = 0; i < 8; ++i) xb[i] = *(const u32x4*)(lds + w * 8320 + (t >> 2) * 1040 + (t & 3) * 256 + i * 32 + hi * 16);
    tag = (((u64)__builtin_amdgcn_readfirstlane(nv[1]) << 32) | (u64)__builtin_amdgcn_readfirstlane(nv[0])) + 1;
    const unsigned trl = (8 * hi + ((lane & 15) >> 2)) * 128 + (((((lane >> 4) & 1) * 16 + (lane & 3) * 4) * 2) ^ (((lane >> 3) & 1) << 6));
    const unsigned trk = (8 * hi + ((lane & 15) >> 2)) * 32 + (lane & 3) * 8;
    const unsigned trlp = trl - 4 * hi * 128, trkp = trk - 4 * hi * 32;
    {
      float s1 = 0.f, s2 = 0.f;
#pragma unroll
      for (int i = 0; i < 8; ++i)
#pragma unroll
        for (int e = 0; e < 4; ++e) { s1 = dot2(xb[i][e], ONES2, s1); s2 = dot2(xb[i][e], xb[i][e], s2); }
      s1 = sum32(s1); s2 = sum32(s2);
      if (hi == 0) { ((f32x2*)(lds + L_ST))[(fx * 2 + ftt) * 32 + t] = (f32x2){s1, s2}; ((f32x2*)(lds + L_ST))[((4 + fx) * 2 + ftt) * 32 + t] = (f32x2){0.f, 0.f}; }
    }
    float* csp = (float*)(lds + L_CSP);
    if (role) {
      BARRIER();
      f32x16 akv = {};
      float cskv = 0.f;
      u32x4 kf[8];
#pragma unroll
      for (int i = 0; i < 8; ++i) { kf[i] = tr_frag_nat(ldsb + L_WKV + fx * 4096 + i * 16 * 32 + trk, 32); cskv = frag_sum(kf[i], cskv); }
      if (role == 1) {
#pragma unroll
        for (int i = 0; i < 8; ++i) akv = mfma32(kf[i], xb[i], akv);
      } else {
#pragma unroll
        for (int i = 0; i < 8; ++i) akv = mfma32(xb[i], kf[i], akv);
      }
      if (G == 0 && ftt == 0) {
#pragma unroll
        for (int i = 0; i < 8; ++i) {
          const u32x4 ap = tr_frag(ldsb + L_WKV + fx * 4096 + i * 16 * 32 + trkp, 32);
          if (((lane >> 4) & 1) == role - 1) st16<SC1>(rs, ap, l16, WKV_T + (8 * fx + i) * 1024);
        }
      }
      cskv = sum32(cskv);
      if (hi == 0 && ftt == 0) csp[256 + fx * 32 + t] = cskv;
      BARRIER();
      char* pw = lds + L_WKV + (ftt * 4 + fx) * 2048;
      if (role == 1) {
        *(f32x4*)(pw + l16) = (f32x4){akv[0], akv[1], akv[2], akv[3]};
        *(f32x4*)(pw + 1024 + l16) = (f32x4){akv[4], akv[5], akv[6], akv[7]};
      } else if (t < 16) {
#pragma unroll
        for (int q = 0; q < 4; ++q) *(f32x4*)(pw + ((q * 2 + hi) * 16 + t) * 16) = (f32x4){akv[4 * q], akv[4 * q + 1], akv[4 * q + 2], akv[4 * q + 3]};
      }
    }
    WAITV0();
    BARRIER();
    if (role && fx == 0) {
      float mean, rstd; ln_stats(st, ftt, t, mean, rstd);
      float* csf = (float*)(lds + L_CSP + 1536 + ftt * 128);
      { const float* p = csp + 256 + t; const float sum = (p[0] + p[32]) + (p[64] + p[96]); if (hi == 0) csf[t] = sum; }
      asm volatile("s_waitcnt lgkmcnt(0)" ::: "memory");
      auto cs = [&](int i) { return csf[i]; };
      auto rd = [&](int s, float* v) {
        const char* pb = lds + L_WKV + ftt * 8192;
        if (role == 1) sum4<2048, 1024>(pb + l16, v); else sum4<2048, 512>(pb + ((4 * s + hi) * 16 + (t & 15)) * 16, v);
      };
      kv_finish(rs, role, rd, mean, rstd, cs, KV0 + (2 * G + ftt) * TB, lane, lds + L_SCR + ftt * 256);
      WAITV0();
      kv_publish(ws, 0, 2 * G + ftt, role, tag, lane);
    }
    u32x4 xr;
    {
      const rsrc_t rx = make_rsrc(x, SEQ * DM * 2);
      const unsigned so = (2 * G + ftt) * 32768 + (4 * j + fx) * 32;
      const u32x2 a = ld8(rx, t * 1024 + hi * 8, so), b = ld8(rx, t * 1024 + hi * 8 + 16, so);
      xr = (u32x4){a[0], a[1], b[0], b[1]};
    }
    f32x16 acc[2] = {};
    float csq0 = 0.f, csq1 = 0.f;
    {
      u32x4 a0[8], a1[8];
#pragma unroll
      for (int i = 0; i < 8; ++i) {
        const unsigned ia = ldsb + L_WO + fx * 16384 + i * 16 * 128;
        a0[i] = tr_frag_nat(ia + trl, 128); a1[i] = tr_frag_nat(ia + (trl ^ 64), 128);
      }
#pragma unroll
      for (int i = 0; i < 8; ++i) {
        acc[0] = mfma32(a0[i], xb[i], acc[0]); acc[1] = mfma32(a1[i], xb[i], acc[1]);
        csq0 = frag_sum(a0[i], csq0); csq1 = frag_sum(a1[i], csq1);
      }
    }
    if (ftt == 0 && (G >> 3) == fx) {
      const unsigned ia = ldsb + L_WO + fx * 16384 + (G & 7) * 16 * 128;
      st16<SC1>(rs, tr_frag(ia + trlp, 128), l16, WQ_T + ((2 * j) * 32 + G) * 1024);
      st16<SC1>(rs, tr_frag(ia + (trlp ^ 64), 128), l16, WQ_T + ((2 * j + 1) * 32 + G) * 1024);
    }
    if (ftt == 0) {
      csq0 = sum32(csq0); csq1 = sum32(csq1);
      if (hi == 0) { csp[fx * 64 + t] = csq0; csp[fx * 64 + 32 + t] = csq1; }
    }
    red4_write<2>(lds + L_RED, w, lane, acc);
    BARRIER();
    {
      float mean, rstd; ln_stats(st, att, t, mean, rstd);
      f32x4 c0 = {}, c1 = {}, c2 = {}, c3 = {};
#pragma unroll
      for (int p = 0; p < 4; ++p) {
        const float* c = csp + p * 64 + 32 * hp + 4 * hi;
        c0 += *(const f32x4*)c; c1 += *(const f32x4*)(c + 8); c2 += *(const f32x4*)(c + 16); c3 += *(const f32x4*)(c + 24);
      }
      float v[8];
      red4_read8<2>(lds + L_RED, att, hp, 0, lane, v); q0 = ln_pack(v, c0, c1, mean, rstd * QSCALE);
      red4_read8<2>(lds + L_RED, att, hp, 1, lane, v); q1 = ln_pack(v, c2, c3, mean, rstd * QSCALE);
    }
    if (tid < 96) {
      const float* p = csp + (tid < 64 ? tid : 256 + tid - 64);
      const int sd = tid < 64 ? 64 : 32;
      ((float*)(lds + L_TAB))[tid] = (p[0] + p[sd]) + (p[2 * sd] + p[3 * sd]);
    }
    if (w == 0) wait_pairs(rs, l16, FLG_KV + (blockIdx.x & 7) * 1024, true, tag);
    BARRIER();
#pragma unroll
    for (int p = 0; p < 5; ++p) DMA(p, KV0, 0);
    *(u32x4*)(lds + L_XR + w * 1024 + l16) = xr;
  }
#pragma unroll 1
  for (int layer = 0; layer < 2; ++layer) {
    const unsigned kvb = KV0 + layer * KVL;
    constexpr int RING = 36, NPRE = 24;
    u32x4 W[RING];
    unsigned wfa, wfs, wfo, wz;
#define WOFF(S) (wfa + (S) * wfs + ((S) & 1) * wfo)
#define WLD(S) do { W[(S) % RING] = ld16<SC1>(rs, l16, (S) < 32 ? WOFF(S) : (WOFF(S) | wz)); } while (0)
#define WLD2(S) do { if ((S) < 64) W[(S) % RING] = ld16<SC1>(rs, l16, WOFF(S)); } while (0)
    f32x16 o0 = {}, o1 = {};
    {
      STAGE_VARS();
      asm volatile("s_waitcnt vmcnt(0) lgkmcnt(0)\n\ts_barrier" ::: "memory");
#pragma unroll
      for (int p = 0; p < 5; ++p) DMA(p, kvb + SLOTB, 1);
      int jn = 0;
      unsigned jvo = 0x80000000u, jld = DM * 2, jdstv = l16, jdst = 0, jcs = 0;
      const uint16_t* jbase = wo; unsigned jbytes = DM * DM * 2;
      if (layer == 0) {
        const int job = blockIdx.x;
        if (job < 88) {
          const int kb = 2 * (job & 3) + (w >> 2), up = (w >> 1) & 1, fb = 4 * (job >> 2) + 2 * (w & 1);
          if (fb < NFB) {
            jn = 4; jld = FF * 2; jbase = up ? wu : wg; jbytes = DM * FF * 2;
            jvo = 64 * kb * (FF * 2) + fb * 32;
            jdst = WGU_T + (fb * 32 + 4 * kb) * 1024; jdstv = (t >> 4) * 32768 + (32 * hi + 16 * up + (t & 15)) * 16;
            jcs = CSGU + ((kb * 86 + fb + (t >> 4)) * 32 + 16 * up + (t & 15)) * 4;
          }
        } else if (job < 152) {
          const bool o = job < 104;
          const int q = o ? job - 88 : job - 104, ct = 2 * (q & 7) + (w & 1), ks0 = 16 * (q >> 3) + 4 * (w >> 1), nks = o ? 32 : 86;
          jn = max(0, min(4, nks - ks0)); jbase = o ? wo : wd; jbytes = o ? DM * DM * 2 : FF * DM * 2;
          if (jn) jvo = 16 * ks0 * (DM * 2) + ct * 64;
          jdst = (o ? WO_T : WD_T) + (ct * nks + ks0) * 1024;
        }
      }
      const unsigned pz_ao = opq(AOF + ((layer * 64 + 2 * G + att) * 32 + 4 * j + 2 * hp + kh) * 1024), pz_x1 = opq(X1F + ((layer * 64 + 2 * G + ftt) * 32 + 4 * j + fx) * 1024);
      const unsigned pz_h0 = opq(HF + ((layer * 64 + 2 * G) * 86 + fb0 + w) * 1024);
      const unsigned pz_h1 = opq((w + 8 < nfb ? 0u : 0x80000000u) | (HF + ((layer * 64 + 2 * G) * 86 + fb0 + w + 8) * 1024));
      const unsigned pz_x2 = opq((layer == 0 ? 0u : 0x80000000u) | (X2F + ((2 * G + ftt) * 32 + 4 * j + fx) * 1024));
#define PZ(OFF) do { unsigned p_ = POISON; asm volatile("" : "+s"(p_)); st16<SC1>(rs, (u32x4){p_, p_, p_, p_}, l16, (OFF)); SB(); } while (0)
      const rsrc_t jrs = make_rsrc(jbase, jbytes);
#define JOBLD() do { const unsigned ld_ = opq(jld); const unsigned vo_ = jvo + (lane >> 2) * ld_ + (lane & 3) * 16; \
          _Pragma("unroll") for (int n_ = 0; n_ < 4; ++n_) glds16<0>(jrs, vo_ + 16 * n_ * ld_, 0, ldsb + L_WO + w * 4096 + n_ * 1024); SB(); } while (0)
      const unsigned ja = ldsb + L_WO + w * 4096 + (4 * hi + ((lane & 15) >> 2)) * 64 + (((lane >> 4) & 1) * 16 + (lane & 3) * 4) * 2;
      const unsigned jcv = (jld == FF * 2 && jn && hi == 0) ? jcs : 0x80000000u;
#define JOBST1(F) do { const u32x4 a_ = tr_frag(ja + (F) * 1024, 64); st16<SC1>(rs, a_, jdstv, ((F) < jn ? 0u : 0x80000000u) | (jdst + (F) * 1024)); cs_ = frag_sum(a_, cs_); } while (0)
#define JOBST() do { WAITV0(); float cs_ = 0.f; JOBST1(0); JOBST1(1); JOBST1(2); JOBST1(3); cs_ = sum32(cs_); \
          __builtin_amdgcn_raw_buffer_store_b32(__float_as_uint(cs_), rs, jcv, 0, SC1); SB(); } while (0)
      SB();
      const int tc = t < 16 ? t : (t == 16 ? 16 : 17);
      const LAS u32x4* kp = (const LAS u32x4*)(size_t)(ldsb + kh * 8 * TB + l16);
      const LAS u32x4* vp = (const LAS u32x4*)(size_t)(ldsb + kh * 8 * TB + 1024 + hi * (VR / 2) + tc * 16);
      const f32x16 z = {};
      f32x16 sA, sB;
      u32x4 kX, vX0, vX1, kY, vY0, vY1, p0, p1;
      kX = kp[0]; vX0 = vp[0]; vX1 = vp[VR / 16];
      sA = mfma32(kX, q0, z);
      vY1 = vX1;
      { unsigned z_ = 0; asm volatile("" : "+v"(z_)); p1 = (u32x4){z_, z_, z_, z_}; }
      float e14 = 0.f, e15 = 0.f;
#define UNIT(SC, SN, KN, QN, OP, VP1, OC, VC0, A1, A2, A3)                                                               \
  do {                                                                                                                   \
    SN = mfma32(KN, QN, z); SB();                                                                                        \
    A1;                                                                                                                  \
    float e0 = EXP2(SC[0]), e1 = EXP2(SC[1]); SB();                                                                      \
    p1[3] = pk2(e14, e15); SB();                                                                                         \
    float e2 = EXP2(SC[2]), e3 = EXP2(SC[3]); SB();                                                                      \
    OP = mfma32(VP1, p1, OP); SB();                                                                                      \
    A2;                                                                                                                  \
    p0[0] = pk2(e0, e1); SB();                                                                                           \
    float e4 = EXP2(SC[4]), e5 = EXP2(SC[5]); SB();                                                                      \
    p0[1] = pk2(e2, e3); SB();                                                                                           \
    float e6 = EXP2(SC[6]), e7 = EXP2(SC[7]); SB();                                                                      \
    p0[2] = pk2(e4, e5); SB();                                                                                           \
    float e8 = EXP2(SC[8]), e9 = EXP2(SC[9]); SB();                                                                      \
    p0[3] = pk2(e6, e7); SB();                                                                                           \
    float e10 = EXP2(SC[10]), e11 = EXP2(SC[11]); SB();                                                                  \
    OC = mfma32(VC0, p0, OC); SB();                                                                                      \
    A3;                                                                                                                  \
    p1[0] = pk2(e8, e9); SB();                                                                                           \
    float e12 = EXP2(SC[12]), e13 = EXP2(SC[13]); SB();                                                                  \
    p1[1] = pk2(e10, e11); SB();                                                                                         \
    e14 = EXP2(SC[14]); e15 = EXP2(SC[15]); SB();                                                                        \
    p1[2] = pk2(e12, e13); SB();                                                                                         \
  } while (0)
#define LDN(KN, VN0, VN1, NOFF) do { KN = kp[(NOFF) / 16]; VN0 = vp[(NOFF) / 16]; VN1 = vp[((NOFF) + VR) / 16]; SB(); } while (0)
#define TILE(KC, VC0, VC1, KN, VN0, VN1, NOFF, X0, X1)                          \
    UNIT(sA, sB, KC, q1, o1, VN1, o0, VC0, X0, LDN(KN, VN0, VN1, NOFF), );      \
    UNIT(sB, sA, KN, q0, o0, VC1, o1, VC0, X1, , );
#define TILES06(SO, H1, H2, H3, H4, H5, H6, H7, H8, H9, H10, H11, H12, H13, H14)  \
      TILE(kX, vX0, vX1, kY, vY0, vY1, (SO) + 1 * TB, H1, H2);                  \
      TILE(kY, vY0, vY1, kX, vX0, vX1, (SO) + 2 * TB, H3, H4);                  \
      TILE(kX, vX0, vX1, kY, vY0, vY1, (SO) + 3 * TB, H5, H6);                  \
      TILE(kY, vY0, vY1, kX, vX0, vX1, (SO) + 4 * TB, H7, H8);                  \
      TILE(kX, vX0, vX1, kY, vY0, vY1, (SO) + 5 * TB, H9, H10);                 \
      TILE(kY, vY0, vY1, kX, vX0, vX1, (SO) + 6 * TB, H11, H12);                \
      TILE(kX, vX0, vX1, kY, vY0, vY1, (SO) + 7 * TB, H13, H14);
#define PINP() asm volatile("" : "+v"(p1), "+v"(e14), "+v"(e15), "+v"(sA), "+v"(o0), "+v"(o1))
#define CHBAR() asm volatile("s_waitcnt vmcnt(0) lgkmcnt(0)\n\ts_barrier" ::: "memory")
#define DM(I, CH, SL) do { glds16<SC1>(rs, l16, (I) < 4 ? dsrc + (CH) * SLOTB + (I) * 8192 : dsrc4 + (CH) * SLOTB, \
                                       (I) < 4 ? ddst + (SL) * SLOTB + (I) * 8192 : (w < 2 ? ddst + (SL) * SLOTB + 4 * 8192 : ldsb + L_DUMMY)); SB(); } while (0)
#define TILE7(SL, A1, A2, A3, B1, B2, B3)                                                        \
      UNIT(sA, sB, kY, q1, o1, vX1, o0, vY0, A1, LDN(kX, vX0, vX1, (1 - (SL)) * SLOTB); A2, A3);  \
      UNIT(sB, sA, kX, q0, o0, vY1, o1, vY0, B1, B2, B3);
      const unsigned dsrc = opq(kvb + w * 1024), dsrc4 = opq(w < 2 ? kvb + w * 1024 + 4 * 8192 : 0x80000000u), ddst = opq(ldsb + w * 1024);
      TILES06(0, , JOBLD(), , , , , , , JOBST(), , , , , );
      CHBAR();
      if (layer == 0 && tid == 0) put_tag((GAS u64*)(ws + FLG_T) + blockIdx.x, tag);
      SB();
      TILE7(0, DM(0, 2, 0); DM(1, 2, 0), DM(2, 2, 0), DM(3, 2, 0), DM(4, 2, 0), , );
      TILES06(SLOTB, PZ(pz_ao), , PZ(pz_x1), , PZ(pz_h0), , PZ(pz_h0 + 86 * 1024), , PZ(pz_h1), , PZ(pz_h1 + 86 * 1024), , PZ(pz_x2), );
      CHBAR();
      if (tid == 0) put_tag((GAS u64*)(ws + FLG_PZ) + (layer * 32 + G) * 8 + j, tag);
      SB();
      TILE7(1, DM(0, 3, 1); DM(1, 3, 1), DM(2, 3, 1), DM(3, 3, 1), DM(4, 3, 1), , );
      u32x4 fl;
      const unsigned chko = FLG_T + w * 256 + (lane & 15) * 16;
#define CHKLD() do { fl = ld16<SC1>(rs, chko, layer == 0 ? 0u : 0x80000000u); SB(); } while (0)
      TILES06(0, , , , , , , , , , CHKLD(), , , , );
      PINP();
      if (layer == 0 && !__all(pair_ok(fl, tag))) wait_pairs(rs, chko, 0, true, tag);
      CHBAR();
      TILE7(0, , , , , , );
      {
        const bool two = w + 8 < nfb;
        wfa = opq(WGU_T + (fb0 + w) * 32 * 1024);
        wfs = opq(two ? 512u : 1024u);
        wfo = opq(two ? 8u * 32u * 1024u - 512u : 0u);
        wz = opq(two ? 0u : 0x80000000u);
      }
      const unsigned wosrc = opq(WO_T + (2 * j) * 32 * 1024 + w * 1024), wodst = opq(ldsb + L_WO + w * 1024);
#define WOD(I) do { glds16<SC1>(rs, l16, wosrc + (I) * 8192, wodst + (I) * 8192); SB(); } while (0)
#define PZLD() do { fl = ld16<SC1>(rs, (lane & 3) * 16, FLG_PZ + (layer * 32 + G) * 64); SB(); } while (0)
#define PRE(U) do { if (2 * (U) < NPRE) { WLD(2 * (U)); WLD(2 * (U) + 1); SB(); } } while (0)
      TILE(kX, vX0, vX1, kY, vY0, vY1, SLOTB + 1 * TB, WOD(0); WOD(1), WOD(2); WOD(3));
      TILE(kY, vY0, vY1, kX, vX0, vX1, SLOTB + 2 * TB, WOD(4); WOD(5), WOD(6); WOD(7));
      TILE(kX, vX0, vX1, kY, vY0, vY1, SLOTB + 3 * TB, PRE(0); PRE(1), PRE(2); PRE(3));
      PINP();
      if (w >= 4) __builtin_amdgcn_s_setprio(1);
      SB();
      TILE(kY, vY0, vY1, kX, vX0, vX1, SLOTB + 4 * TB, PRE(4); PRE(5), PRE(6); PRE(7));
      TILE(kX, vX0, vX1, kY, vY0, vY1, SLOTB + 5 * TB, PRE(8); PRE(9), PRE(10));
      TILE(kY, vY0, vY1, kX, vX0, vX1, SLOTB + 6 * TB, PRE(11), PRE(12));
      TILE(kX, vX0, vX1, kY, vY0, vY1, SLOTB + 7 * TB, PRE(13), PZLD());
      UNIT(sA, sB, kY, q1, o1, vX1, o0, vY0, , , );
      UNIT(sB, sA, kY, q0, o0, vY1, o1, vY0, , , );
      p1[3] = pk2(e14, e15);
      o1 = mfma32(vY1, p1, o1);
      if (!__all(pair_ok(fl, tag))) wait_pairs(rs, (lane & 3) * 16, FLG_PZ + (layer * 32 + G) * 64, true, tag);
    }
    __builtin_amdgcn_s_setprio(0);
    {
      STAGE_VARS();
      const int tc = t < 16 ? t : (t == 16 ? 16 : 17);
      f32x4* dst = (f32x4*)(lds + w * 3072);
      if (kh) { dst[lane] = (f32x4){o0[0], o0[1], o0[2], o0[3]}; dst[64 + lane] = (f32x4){o0[4], o0[5], o0[6], o0[7]}; dst[128 + lane] = (f32x4){o0[8], 0.f, 0.f, 0.f}; }
      else    { dst[lane] = (f32x4){o1[0], o1[1], o1[2], o1[3]}; dst[64 + lane] = (f32x4){o1[4], o1[5], o1[6], o1[7]}; dst[128 + lane] = (f32x4){o1[8], 0.f, 0.f, 0.f}; }
      BARRIER();
      const f32x4* src = (const f32x4*)(lds + (w ^ 4) * 3072);
      const f32x4 a = src[lane], b = src[64 + lane], cc = src[128 + lane];
      float k8[9];
#pragma unroll
      for (int i = 0; i < 9; ++i) k8[i] = kh ? o1[i] : o0[i];
      float r[8] = {k8[0] + a[0], k8[1] + a[1], k8[2] + a[2], k8[3] + a[3], k8[4] + b[0], k8[5] + b[1], k8[6] + b[2], k8[7] + b[3]};
      float l = sum32(k8[8] + cc[0]);
      const bool ok = (l >= 0x1p-60f) && (l <= 0x1p60f);
      if (!__all(ok)) {
        const u32x4 qf = kh ? q1 : q0;
        f32x16 o = {};
        float m = -INFINITY;
        for (int tile = 0; tile < NRB; ++tile) {
          const u32x4 kf = ld16<SC1>(rs, l16, kvb + tile * TB);
          const u32x4 v0 = ld16<SC1>(rs, hi * (VR / 2) + tc * 16, kvb + tile * TB + 1024);
          const u32x4 v1 = ld16<SC1>(rs, hi * (VR / 2) + tc * 16, kvb + tile * TB + 1024 + VR);
          const f32x16 z = {};
          const f32x16 s = mfma32(kf, qf, z);
          float mx = s[0];
#pragma unroll
          for (int i = 1; i < 16; ++i) mx = fmaxf(mx, s[i]);
          mx = fmaxf(max32(mx), m);
          const float f = EXP2(m - mx);
          m = mx;
#pragma unroll
          for (int i = 0; i < 16; ++i) o[i] *= f;
          u32x4 p0, p1;
#pragma unroll
          for (int e = 0; e < 4; ++e) {
            p0[e] = pk2(EXP2(s[2 * e] - m), EXP2(s[2 * e + 1] - m));
            p1[e] = pk2(EXP2(s[8 + 2 * e] - m), EXP2(s[8 + 2 * e + 1] - m));
          }
          o = mfma32(v0, p0, o);
          o = mfma32(v1, p1, o);
        }
        l = sum32(o[8]);
#pragma unroll
        for (int i = 0; i < 8; ++i) r[i] = o[i];
      }
      const float il = 1.f / l;
      const u32x4 ao = {pk2(r[0] * il, r[1] * il), pk2(r[2] * il, r[3] * il), pk2(r[4] * il, r[5] * il), pk2(r[6] * il, r[7] * il)};
      SB();
      st16<SC1>(rs, unpoison(ao), l16, AOF + ((layer * 64 + 2 * G + att) * 32 + 4 * j + 2 * hp + kh) * 1024);
      SB();
    }
    {
      STAGE_VARS();
      if (layer == 0) {
        if (tid < nfb * 32) {
          float p[8];
#pragma unroll
          for (int k = 0; k < 8; ++k) p[k] = __uint_as_float(__builtin_amdgcn_raw_buffer_load_b32(rs, tid * 4, CSGU + (k * 86 + fb0) * 128, SC1));
          ((float*)(lds + L_CSGU))[tid] = ((p[0] + p[1]) + (p[2] + p[3])) + ((p[4] + p[5]) + (p[6] + p[7]));
        }
      }
      const unsigned ab = opq(AOF + ((layer * 64 + 2 * G + ftt) * 32 + 8 * fx) * 1024);
      u32x4 b[8];
      poll_frags1<8>(rs, l16, ab, b);
      WAITV0();
      BARRIER();
      f32x16 acc[2] = {};
#pragma unroll
      for (int i = 0; i < 8; ++i) {
        const u32x4 a0 = *(const u32x4*)(lds + L_WO + (8 * fx + i) * 1024 + l16), a1 = *(const u32x4*)(lds + L_WO + (32 + 8 * fx + i) * 1024 + l16);
        acc[0] = mfma32(a0, b[i], acc[0]); acc[1] = mfma32(a1, b[i], acc[1]);
      }
      asm volatile("" : "+v"(acc[0]), "+v"(acc[1]));
      red4_write<2>(lds + L_RED, w, lane, acc);
      BARRIER();
      float v[8]; red4_read8<2>(lds + L_RED, ftt, fx >> 1, fx & 1, lane, v);
      u32x4 xr = *(const u32x4*)(lds + L_XR + w * 1024 + l16);
#pragma unroll
      for (int e = 0; e < 4; ++e) xr[e] = pk2(v[2 * e] + bflo(xr[e]), v[2 * e + 1] + bfhi(xr[e]));
      xr = unpoison(xr);
      *(u32x4*)(lds + L_XR + w * 1024 + l16) = xr;
      asm volatile("" : "+v"(xr));
      SB();
      st16<SC1>(rs, xr, l16, X1F + ((layer * 64 + 2 * G + ftt) * 32 + 4 * j + fx) * 1024);
      BARRIER();
#pragma unroll
      for (int n = NPRE; n < RING; ++n) WLD(n);
      SB();
    }
    {
      STAGE_VARS();
      const unsigned xb = opq(X1F + ((layer * 64 + 2 * G) * 32 + 4 * w) * 1024);
      {
        u32x4 b[8];
        poll_frags<4, 32768>(rs, l16, xb, b);
#pragma unroll
        for (int tt = 0; tt < 2; ++tt) {
          float s1 = 0.f, s2 = 0.f;
#pragma unroll
          for (int i = 0; i < 4; ++i) {
            *(u32x4*)(lds + L_RED + (tt * 32 + 4 * w + i) * 1024 + l16) = b[tt * 4 + i];
#pragma unroll
            for (int e = 0; e < 4; ++e) { s1 = dot2(b[tt * 4 + i][e], ONES2, s1); s2 = dot2(b[tt * 4 + i][e], b[tt * 4 + i][e], s2); }
          }
          s1 = sum32(s1); s2 = sum32(s2);
          if (hi == 0) ((f32x2*)(lds + L_ST))[(w * 2 + tt) * 32 + t] = (f32x2){s1, s2};
        }
      }
      BARRIER();
      {
        const int k0 = w < 6 ? 11 * w : 66 + 10 * (w - 6);
        const unsigned db = opq(WD_T + ((2 * j) * 86 + k0) * 1024);
#pragma unroll
        for (int i = 0; i < 8; ++i) glds16<SC1>(rs, l16, db + ((i & 1) * 86 + (i >> 1)) * 1024, ldsb + L_WO + w * 8192 + i * 1024);
        SB();
      }
      float rstd[2], mr[2];
#pragma unroll
      for (int tt = 0; tt < 2; ++tt) { float mean; ln_stats(st, tt, t, mean, rstd[tt]); mr[tt] = mean * rstd[tt]; }
      const unsigned hb0 = opq(HF + ((layer * 64 + 2 * G) * 86 + fb0 + w) * 1024);
      const float* csb = (const float*)(lds + L_CSGU) + w * 32 + 4 * hi;
      const f32x16 zf = {};
#define XB(TT, KS) (*(const u32x4*)(lds + L_RED + ((TT) * 32 + (KS)) * 1024 + l16))
#define EPI(A0, A1, BLK) do {                                                                           \
        const float* cs_ = csb + (BLK) * 256;                                                           \
        const f32x4 cg0 = *(const f32x4*)cs_, cg1 = *(const f32x4*)(cs_ + 8), cu0 = *(const f32x4*)(cs_ + 16), cu1 = *(const f32x4*)(cs_ + 24); \
        st16<SC1>(rs, unpoison(geglu8(A0, cg0, cg1, cu0, cu1, rstd[0], mr[0])), l16, hb0 + (BLK) * 8192);             \
        st16<SC1>(rs, unpoison(geglu8(A1, cg0, cg1, cu0, cu1, rstd[1], mr[1])), l16, hb0 + 86 * 1024 + (BLK) * 8192); \
      } while (0)
#define FPASS(NB, DP) do {                                                                              \
        f32x16 a00, a01, a10, a11;                                                                      \
        u32x4 bq0[DP], bq1[DP];                                                                         \
        _Pragma("unroll") for (int i = 0; i < (DP); ++i) { bq0[i] = XB(0, i); bq1[i] = XB(1, i); }      \
        _Pragma("unroll") for (int ks = 0; ks < 32; ++ks) {                                             \
          const int s_ = (NB) * ks;                                                                     \
          a00 = mfma32(W[s_ % RING], bq0[ks % (DP)], ks == 0 ? zf : a00);                               \
          a01 = mfma32(W[s_ % RING], bq1[ks % (DP)], ks == 0 ? zf : a01);                               \
          if ((NB) == 2) {                                                                              \
            a10 = mfma32(W[(s_ + 1) % RING], bq0[ks % (DP)], ks == 0 ? zf : a10);                       \
            a11 = mfma32(W[(s_ + 1) % RING], bq1[ks % (DP)], ks == 0 ? zf : a11);                       \
          }                                                                                             \
          SB();                                                                                         \
          if (ks + (DP) < 32) { bq0[ks % (DP)] = XB(0, ks + (DP)); bq1[ks % (DP)] = XB(1, ks + (DP)); } \
          if ((NB) == 2) { WLD2(s_ + RING); WLD2(s_ + 1 + RING); }                                      \
          SB();                                                                                         \
        }                                                                                               \
        asm volatile("" : "+v"(a00), "+v"(a01));                                                        \
        EPI(a00, a01, 0);                                                                               \
        if ((NB) == 2) EPI(a10, a11, 1);                                                                \
      } while (0)
      if (w + 8 < nfb) { FPASS(2, 3); } else { __builtin_amdgcn_s_setprio(1); FPASS(1, 6); __builtin_amdgcn_s_setprio(0); }
    }
    {
      STAGE_VARS();
      const int k0 = w < 6 ? 11 * w : 66 + 10 * (w - 6), nk = w < 6 ? 11 : 10;
      const unsigned hb = opq(HF + ((layer * 64 + 2 * G) * 86 + k0) * 1024);
      const unsigned db = opq(WD_T + ((2 * j) * 86 + k0) * 1024), zlast = opq(w < 6 ? 0u : 0x80000000u);
      BARRIER();
      u32x4 wd_[14];
      prepoll<86 * 1024>(rs, hb, nk, lane);
      f32x16 acc[4] = {};
      u32x4 b0[22];
#define LB(I0, N) do { _Pragma("unroll") for (int i = 2 * (I0); i < 2 * ((I0) + (N)); ++i) \
          b0[i] = ld16<SC1>(rs, l16, ((i >> 1) < 10 ? 0u : zlast) | (hb + (i & 1) * 86 * 1024 + (i >> 1) * 1024)); } while (0)
#define WDF(S) ((S) < 8 ? *(const u32x4*)(lds + L_WO + w * 8192 + (S) * 1024 + l16) : wd_[(S) < 8 ? 0 : (S) - 8])
#define MM(I0, N) do { _Pragma("unroll") for (int ii = (I0); ii < (I0) + (N); ++ii) { \
          const u32x4 a0_ = WDF(2 * ii), a1_ = WDF(2 * ii + 1); \
          acc[0] = mfma32(a0_, b0[2 * ii], acc[0]); acc[1] = mfma32(a0_, b0[2 * ii + 1], acc[1]); \
          acc[2] = mfma32(a1_, b0[2 * ii], acc[2]); acc[3] = mfma32(a1_, b0[2 * ii + 1], acc[3]); SB(); } } while (0)
#define CK(I0, N) do { if (__builtin_expect(!frags_ok<2 * (N)>(b0 + 2 * (I0)), 0)) { \
          for (unsigned spins = 0; spins < SPIN_CAP; ++spins) { asm volatile("" ::: "memory"); LB(I0, N); if (frags_ok<2 * (N)>(b0 + 2 * (I0))) break; } } } while (0)
      LB(0, 11);
      BARRIER();
#pragma unroll
      for (int i = 8; i < 22; ++i) wd_[i - 8] = ld16<SC1>(rs, l16, ((i >> 1) < 10 ? 0u : zlast) | (db + ((i & 1) * 86 + (i >> 1)) * 1024));
      SB();
      CK(0, 4);
      MM(0, 4);
      CK(4, 4);
      MM(4, 4);
      CK(8, 3);
      MM(8, 3);
      asm volatile("" : "+v"(acc[0]), "+v"(acc[1]), "+v"(acc[2]), "+v"(acc[3]));
      BARRIER();
      red_write(lds + L_RED, w, lane, acc);
      BARRIER();
      float v[8]; red_read8(lds + L_RED, (fx >> 1) * 2 + ftt, fx & 1, lane, v);
      u32x4 xr = *(const u32x4*)(lds + L_XR + w * 1024 + l16);
#pragma unroll
      for (int e = 0; e < 4; ++e) xr[e] = pk2(v[2 * e] + bflo(xr[e]), v[2 * e + 1] + bfhi(xr[e]));
      if (layer == 0) {
        xr = unpoison(xr);
        *(u32x4*)(lds + L_XR + w * 1024 + l16) = xr;
        st16<SC1>(rs, xr, l16, X2F + ((2 * G + ftt) * 32 + 4 * j + fx) * 1024);
      } else {
        uint16_t* p = out + (size_t)(64 * G + 32 * ftt + t) * DM + 16 * (4 * j + fx) + 4 * hi;
        *(u32x2*)p = (u32x2){xr[0], xr[1]};
        *(u32x2*)(p + 8) = (u32x2){xr[2], xr[3]};
        if (blockIdx.x == 0 && tid == 0) put_tag((GAS u64*)(ws + NONCE), tag);
      }
    }
    if (layer == 0) {
      STAGE_VARS();
      u32x4 wqr[16], wkr[8];
      BARRIER();
      const unsigned qb = opq(WQ_T + ((2 * j) * 32 + 8 * fx) * 1024), kb = opq((role ? 0u : 0x80000000u) | (WKV_T + 8 * fx * 1024));
#pragma unroll
      for (int i = 0; i < 8; ++i) wkr[i] = ld16<SC1>(rs, l16, kb + i * 1024);
#pragma unroll
      for (int i = 0; i < 8; ++i) { wqr[2 * i] = ld16<SC1>(rs, l16, qb + i * 1024); wqr[2 * i + 1] = ld16<SC1>(rs, l16, qb + (32 + i) * 1024); }
      SB();
      const unsigned x2b = opq(X2F + ((2 * G + ftt) * 32 + 8 * fx) * 1024);
      u32x4 b[8];
      poll_frags1<8>(rs, l16, x2b, b);
      f32x16 acc[3] = {};
      if (role == 1) {
#pragma unroll
        for (int i = 0; i < 8; ++i) acc[2] = mfma32(wkr[i], b[i], acc[2]);
      } else if (role == 2) {
#pragma unroll
        for (int i = 0; i < 8; ++i) acc[2] = mfma32(b[i], wkr[i], acc[2]);
      }
      float s1 = 0.f, s2 = 0.f;
#pragma unroll
      for (int i = 0; i < 8; ++i) {
        acc[0] = mfma32(wqr[2 * i], b[i], acc[0]); acc[1] = mfma32(wqr[2 * i + 1], b[i], acc[1]);
#pragma unroll
        for (int e = 0; e < 4; ++e) { s1 = dot2(b[i][e], ONES2, s1); s2 = dot2(b[i][e], b[i][e], s2); }
      }
      s1 = sum32(s1); s2 = sum32(s2);
      if (hi == 0) { ((f32x2*)(lds + L_ST))[(fx * 2 + ftt) * 32 + t] = (f32x2){s1, s2}; ((f32x2*)(lds + L_ST))[((4 + fx) * 2 + ftt) * 32 + t] = (f32x2){0.f, 0.f}; }
      red4_write<3>(lds + L_RED, w, lane, acc);
      BARRIER();
      if (role && fx == 0) {
        float mean, rstd; ln_stats(st, ftt, t, mean, rstd);
        const float* ck = (const float*)(lds + L_CSKV);
        auto cs = [&](int i) { return ck[i]; };
        auto rd = [&](int s, float* v) { red4_read8<3>(lds + L_RED, ftt, 2, s, role == 1 ? lane : (lane | 16), v); };
        kv_finish(rs, role, rd, mean, rstd, cs, KV0 + KVL + (2 * G + ftt) * TB, lane, lds + L_SCR + ftt * 256);
        WAITV0();
        kv_publish(ws, 1, 2 * G + ftt, role, tag, lane);
      }
      {
        float mean, rstd; ln_stats(st, att, t, mean, rstd);
        float v[8];
        const float* c = (const float*)(lds + L_CSQ) + 32 * hp + 4 * hi;
        red4_read8<3>(lds + L_RED, att, hp, 0, lane, v);
        q0 = ln_pack(v, *(const f32x4*)c, *(const f32x4*)(c + 8), mean, rstd * QSCALE);
        red4_read8<3>(lds + L_RED, att, hp, 1, lane, v);
        q1 = ln_pack(v, *(const f32x4*)(c + 16), *(const f32x4*)(c + 24), mean, rstd * QSCALE);
      }
      if (w == 0) wait_pairs(rs, l16, FLG_KV + (8 + (blockIdx.x & 7)) * 1024, true, tag);
      BARRIER();
#pragma unroll
      for (int p = 0; p < 5; ++p) DMA(p, KV0 + KVL, 0);
    }
  }
}

extern "C" void kernel_launch(void* const* d_in, const int* in_sizes, int n_in, void* d_out, int out_size, void* d_ws,
                              size_t ws_size, hipStream_t stream) {
  const uint16_t* x = (const uint16_t*)d_in[0];
  const uint16_t* wq = (const uint16_t*)d_in[1];
  const uint16_t* wk = (const uint16_t*)d_in[2];
  const uint16_t* wv = (const uint16_t*)d_in[3];
  const uint16_t* wo = (const uint16_t*)d_in[4];
  const uint16_t* wg = (const uint16_t*)d_in[6];
  const uint16_t* wu = (const uint16_t*)d_in[7];
  const uint16_t* wd = (const uint16_t*)d_in[8];
  (void)hipFuncSetAttribute((const void*)k_main, hipFuncAttributeMaxDynamicSharedMemorySize, L_END);
  k_main<<<256, 512, L_END, stream>>>(x, wq, wk, wv, wo, wg, wu, wd, (char*)d_ws, (uint16_t*)d_out);
}
```
